```python
import torch

def _softmax(x, axis=-1):
    return torch.softmax(x, dim=axis)

def _gelu(x):
    return torch.nn.functional.gelu(x, approximate='tanh')

def _silu(x):
    return torch.nn.functional.silu(x)

def _sigmoid(x):
    return torch.sigmoid(x)

def _layernorm(x, axis=-1, eps=1e-05):
    m = x.mean(dim=axis, keepdim=True)
    v = x.var(dim=axis, keepdim=True, unbiased=False)
    return (x - m) / torch.sqrt(v + eps)

def _scatter_add(tgt, idx, src, axis=0):
    return tgt.clone().index_add_(axis, idx, src)

def _segment_sum(src, seg, n):
    out = torch.zeros((n,) + src.shape[1:], dtype=src.dtype)
    return out.index_add_(0, seg, src)

def _pad(x, pw):
    flat = tuple((v for pair in reversed(pw) for v in pair))
    return torch.nn.functional.pad(x, flat)
kernel_name = 'adaLN_cond__softmax_full_mha__swiglu_ffn__prompt'

def setup_inputs():

    _w = lambda a: (lambda t: t.to(torch.bfloat16) if t.is_floating_point() else t)(torch.as_tensor(a))

    def _A_setup():
        B, S, D = (1, 2048, 512)
        return {'x': _w((lambda *s: torch.randn(*s, dtype=torch.bfloat16) * 0.05)(B, S, D)), 'wqkv': _w((lambda *s: torch.randn(*s, dtype=torch.bfloat16) * 0.05)(D, 3 * D)), 'wo': _w((lambda *s: torch.randn(*s, dtype=torch.bfloat16) * 0.05)(D, D))}
    _A_in = _A_setup()

    def _B_setup():
        B, S, D, F = (1, 2048, 512, 1792)
        return {'x': _w((lambda *s: torch.randn(*s, dtype=torch.bfloat16) * 0.05)(B, S, D)), 'w_gate': _w((lambda *s: torch.randn(*s, dtype=torch.bfloat16) * 0.05)(D, F)), 'w_up': _w((lambda *s: torch.randn(*s, dtype=torch.bfloat16) * 0.05)(D, F)), 'w_down': _w((lambda *s: torch.randn(*s, dtype=torch.bfloat16) * 0.05)(F, D))}
    _B_in = _B_setup()
    return {'A_x': _A_in['x'], 'A_wqkv': _A_in['wqkv'], 'A_wo': _A_in['wo'], 'B_x': _B_in['x'], 'B_w_gate': _B_in['w_gate'], 'B_w_up': _B_in['w_up'], 'B_w_down': _B_in['w_down'], 'c': _w((lambda *s: torch.randn(*s, dtype=torch.bfloat16) * 0.1)(1, 512) * 8.0), 'w_mod': _w((lambda *s: torch.randn(*s, dtype=torch.bfloat16) * 0.1)(512, 6 * 512) * 512 ** (-0.5) * 8.0)}

def reference(A_x, A_wqkv, A_wo, B_x, B_w_gate, B_w_up, B_w_down, c, w_mod):

    def _A_ref(x, wqkv, wo):
        B, S, D = x.shape
        H = 32
        Dh = D // H
        qkv = (lambda x, *d: x.permute(*d))((x @ wqkv).reshape(B, S, 3, H, Dh), 2, 0, 3, 1, 4)
        q, k, v = (qkv[0], qkv[1], qkv[2])
        scale = Dh ** (-0.5)
        scores = torch.einsum('bhqd,bhkd->bhqk', q, k) * scale
        p = _softmax(scores, axis=-1)
        o = torch.einsum('bhqk,bhkd->bhqd', p, v)
        return (lambda x, *d: x.permute(*d))(o, 0, 2, 1, 3).reshape(B, S, D) @ wo

    def _B_ref(x, w_gate, w_up, w_down):
        g = _silu(x @ w_gate)
        u = x @ w_up
        return g * u @ w_down
    x = A_x
    mod = (_silu(c) @ w_mod).reshape(c.shape[0], 6, 1, -1)
    sa, ca, ga, sb, cb, gb = (mod[:, 0].clone(), mod[:, 1].clone(), mod[:, 2].clone(), mod[:, 3].clone(), mod[:, 4].clone(), mod[:, 5].clone())
    h = _layernorm(x, axis=-1) * (1 + ca) + sa
    x = x + ga * _A_ref(h, A_wqkv, A_wo)
    h = _layernorm(x, axis=-1) * (1 + cb) + sb
    x = x + gb * _B_ref(h, B_w_gate, B_w_up, B_w_down)
    return x
```

```cpp
#include <hip/hip_runtime.h>
#include <hip/hip_bf16.h>
#include <stdint.h>

typedef __attribute__((ext_vector_type(8))) __bf16 bf16x8;
typedef __attribute__((ext_vector_type(2))) __bf16 bf16x2;
typedef __attribute__((ext_vector_type(2))) float f32x2;
typedef __attribute__((ext_vector_type(4))) float f32x4;
typedef __attribute__((ext_vector_type(16))) float f32x16;
typedef __attribute__((ext_vector_type(4))) unsigned u32x4;
typedef __attribute__((ext_vector_type(2))) unsigned u32x2;
typedef unsigned short u16;
#define LAS __attribute__((address_space(3)))

constexpr int S_ = 2048, D_ = 512, F_ = 1792;
constexpr int KVT = 2080;
constexpr int KVH = 64 * KVT;
constexpr size_t WS_MOD = 0;
constexpr size_t WS_C12 = 16384;
constexpr size_t WS_STAT = WS_C12 + 65536;
constexpr size_t WS_XH = WS_STAT + 262144;
constexpr size_t WS_WQKV = WS_XH + 2097152;
constexpr size_t WS_Q = WS_WQKV + 1572864;
constexpr size_t WS_KV = WS_Q + 2097152;
constexpr size_t WS_O = WS_KV + 32 * (size_t)KVH;
constexpr size_t WS_WO = WS_O + 2097152;
constexpr size_t WS_WGU = WS_WO + 524288;
constexpr size_t WS_WD = WS_WGU + 3670016;
constexpr size_t WS_X1 = WS_WD + 1835008;
constexpr size_t WS_Y = WS_X1 + 2097152;
constexpr size_t WS_ACT = WS_Y + 2097152;

__device__ __forceinline__ float bflo(unsigned u) { return __uint_as_float(u << 16); }
__device__ __forceinline__ float bfhi(unsigned u) { return __uint_as_float(u & 0xffff0000u); }
__device__ __forceinline__ unsigned pk2(float a, float b) {
    f32x2 f = {a, b};
    return __builtin_bit_cast(unsigned, __builtin_convertvector(f, bf16x2));
}
__device__ __forceinline__ float rbf(float a) { return (float)(__bf16)a; }
__device__ __forceinline__ f32x16 mfma32(u32x4 a, u32x4 b, f32x16 c) {
    return __builtin_amdgcn_mfma_f32_32x32x16_bf16(__builtin_bit_cast(bf16x8, a), __builtin_bit_cast(bf16x8, b), c, 0, 0, 0);
}
__device__ __forceinline__ float xhalf(float v) {
    auto r = __builtin_amdgcn_permlane32_swap(__float_as_uint(v), __float_as_uint(v), false, false);
    return (threadIdx.x & 32) ? __uint_as_float(r[0]) : __uint_as_float(r[1]);
}
__device__ __forceinline__ void glds16(const void* g, unsigned lds_base) {
    unsigned sv;
    asm volatile("s_mov_b32 %0, m0\n\ts_mov_b32 m0, %2\n\ts_nop 0\n\tglobal_load_lds_dwordx4 %1, off\n\ts_mov_b32 m0, %0"
                 : "=&s"(sv) : "v"(g), "s"(lds_base) : "memory");
}
#define BAR() asm volatile("s_waitcnt lgkmcnt(0)\n\ts_barrier" ::: "memory")
#define WAITV_BAR(N) asm volatile("s_waitcnt vmcnt(" #N ") lgkmcnt(0)\n\ts_barrier" ::: "memory")
__device__ __forceinline__ u32x4 lds_rd(const char* base, int off) { return *(const LAS u32x4*)((const LAS char*)base + off); }

__device__ __forceinline__ u32x4 gather_frag(const u16* p, int ld) {
    u32x4 r;
    r[0] = (unsigned)p[0] | ((unsigned)p[ld] << 16);
    r[1] = (unsigned)p[2 * ld] | ((unsigned)p[3 * ld] << 16);
    r[2] = (unsigned)p[8 * ld] | ((unsigned)p[9 * ld] << 16);
    r[3] = (unsigned)p[10 * ld] | ((unsigned)p[11 * ld] << 16);
    return r;
}

__global__ __launch_bounds__(512) void k0_prep(const u16* __restrict__ x, const u16* __restrict__ wqkv, const u16* __restrict__ c,
                                               const u16* __restrict__ wmod, char* __restrict__ ws) {
    __shared__ float red[8][32];
    const int b = blockIdx.x, tid = threadIdx.x, lane = tid & 63, w = tid >> 6;
    if (b < 96) {
        const int ch = lane & 3, r = lane >> 2;
        u32x4 wv[4]; float sc[4];
#pragma unroll
        for (int i = 0; i < 4; ++i) {
            const int row = 64 * w + 16 * i + r;
            wv[i] = *(const u32x4*)(wmod + (size_t)row * 3072 + 32 * b + 8 * ch);
            sc[i] = bflo(c[row]);
        }
        float acc[8];
#pragma unroll
        for (int j = 0; j < 8; ++j) acc[j] = 0.f;
#pragma unroll
        for (int i = 0; i < 4; ++i) {
            const float s = rbf(sc[i] / (1.f + __expf(-sc[i])));
#pragma unroll
            for (int j = 0; j < 4; ++j) { acc[2 * j] += s * bflo(wv[i][j]); acc[2 * j + 1] += s * bfhi(wv[i][j]); }
        }
#pragma unroll
        for (int j = 0; j < 8; ++j) {
            acc[j] += __shfl_xor(acc[j], 4); acc[j] += __shfl_xor(acc[j], 8); acc[j] += __shfl_xor(acc[j], 16); acc[j] += __shfl_xor(acc[j], 32);
        }
        if (lane < 4) {
#pragma unroll
            for (int j = 0; j < 8; ++j) red[w][8 * ch + j] = acc[j];
        }
        __syncthreads();
        if (tid < 32) {
            float s = 0.f;
#pragma unroll
            for (int i = 0; i < 8; ++i) s += red[i][tid];
            ((float*)(ws + WS_MOD))[32 * b + tid] = rbf(s);
        }
    } else if (b < 160) {
        const int tt = b - 96, r = lane & 3, cc = lane >> 2, tok = 4 * w + r;
        const u16* xr = x + (size_t)(32 * tt + tok) * D_;
        u32x4 v[4];
        v[0] = *(const u32x4*)(xr + 16 * cc); v[1] = *(const u32x4*)(xr + 16 * cc + 8);
        v[2] = *(const u32x4*)(xr + 16 * cc + 256); v[3] = *(const u32x4*)(xr + 16 * cc + 264);
        float f[32];
#pragma unroll
        for (int i = 0; i < 4; ++i)
#pragma unroll
            for (int j = 0; j < 4; ++j) { f[8 * i + 2 * j] = bflo(v[i][j]); f[8 * i + 2 * j + 1] = bfhi(v[i][j]); }
        float s = 0.f;
#pragma unroll
        for (int i = 0; i < 32; ++i) s += f[i];
        s += __shfl_xor(s, 4); s += __shfl_xor(s, 8); s += __shfl_xor(s, 16); s += __shfl_xor(s, 32);
        const float mean = s * (1.f / 512.f);
        float q = 0.f;
#pragma unroll
        for (int i = 0; i < 32; ++i) { f[i] -= mean; q += f[i] * f[i]; }
        q += __shfl_xor(q, 4); q += __shfl_xor(q, 8); q += __shfl_xor(q, 16); q += __shfl_xor(q, 32);
        const float rs = __builtin_amdgcn_rsqf(q * (1.f / 512.f) + 1e-5f);
        char* dst = ws + WS_XH + (size_t)tt * 32768 + tok * 16;
#pragma unroll
        for (int p = 0; p < 2; ++p) {
            const float* g = f + 16 * p;
            u32x4 h0 = {pk2(g[0] * rs, g[1] * rs), pk2(g[2] * rs, g[3] * rs), pk2(g[8] * rs, g[9] * rs), pk2(g[10] * rs, g[11] * rs)};
            u32x4 h1 = {pk2(g[4] * rs, g[5] * rs), pk2(g[6] * rs, g[7] * rs), pk2(g[12] * rs, g[13] * rs), pk2(g[14] * rs, g[15] * rs)};
            *(u32x4*)(dst + (cc + 16 * p) * 1024) = h0;
            *(u32x4*)(dst + (cc + 16 * p) * 1024 + 512) = h1;
        }
    } else {
        const int f0 = (b - 160) * 16 + 2 * w;
        u32x4 fr[2];
#pragma unroll
        for (int i = 0; i < 2; ++i) {
            const int f = f0 + i, nt = f >> 5, ks = f & 31;
            fr[i] = gather_frag(wqkv + (size_t)(16 * ks + 4 * (lane >> 5)) * 1536 + 32 * nt + (lane & 31), 1536);
        }
#pragma unroll
        for (int i = 0; i < 2; ++i) *(u32x4*)(ws + WS_WQKV + (size_t)(f0 + i) * 1024 + lane * 16) = fr[i];
    }
}

constexpr int P1_LDS = 98304 + 4096;
__global__ __launch_bounds__(512) void p1_qkv(char* __restrict__ ws) {
    extern __shared__ __attribute__((aligned(16))) char lds[];
    const int b = blockIdx.x, tid = threadIdx.x, lane = tid & 63;
    const int w = __builtin_amdgcn_readfirstlane(tid >> 6);
    const int cp = 2 * (b & 7) + ((b >> 3) & 1), rp = b >> 4;
    if (w >= 4) {
        const unsigned l0 = (unsigned)(uintptr_t)lds;
        const char* src = ws + WS_WQKV + (size_t)cp * 32768 + lane * 16;
        const int h = w - 4;
#pragma unroll
        for (int i = 0; i < 24; ++i) {
            const int p = 4 * i + h, ks = p / 3, wh = p - 3 * ks;
            glds16(src + wh * (16 * 32768) + ks * 1024, l0 + p * 1024);
        }
        WAITV_BAR(18);
        WAITV_BAR(0);
        return;
    }
    const float* mod = (const float*)(ws + WS_MOD);
    const f32x2 sa2 = *(const f32x2*)(mod + 2 * tid), ca2 = *(const f32x2*)(mod + 512 + 2 * tid);
    const int tt = 4 * rp + w;
    const char* xs = ws + WS_XH + (size_t)tt * 32768 + lane * 16;
    u32x4 xf[32];
#pragma unroll
    for (int ks = 0; ks < 32; ++ks) xf[ks] = *(const u32x4*)(xs + ks * 1024);
    {
        const int k = 2 * tid, ks = k >> 4, hh = (k >> 2) & 1, j = ((k >> 3) & 1) * 4 + (k & 3);
        float* tab = (float*)(lds + 98304) + (ks * 2 + hh) * 16 + j;
        f32x2 g2 = {rbf(1.f + ca2[0]), rbf(1.f + ca2[1])};
        *(f32x2*)tab = g2; *(f32x2*)(tab + 8) = sa2;
    }
    f32x16 aq = {}, ak = {}, av = {};
    const int hh = lane >> 5;
    BAR();
#pragma unroll
    for (int ks = 0; ks < 32; ++ks) {
        if (ks == 8) BAR();
        const LAS f32x4* t = (const LAS f32x4*)((const LAS char*)lds + 98304 + (ks * 2) * 64 + hh * 64);
        const f32x4 g0 = t[0], g1 = t[1], s0 = t[2], s1 = t[3];
        const u32x4 wq = lds_rd(lds, ks * 3072 + lane * 16), wk = lds_rd(lds, ks * 3072 + 1024 + lane * 16), wv = lds_rd(lds, ks * 3072 + 2048 + lane * 16);
        const u32x4 xv = xf[ks];
        u32x4 hb;
        hb[0] = pk2(fmaf(bflo(xv[0]), g0[0], s0[0]), fmaf(bfhi(xv[0]), g0[1], s0[1]));
        hb[1] = pk2(fmaf(bflo(xv[1]), g0[2], s0[2]), fmaf(bfhi(xv[1]), g0[3], s0[3]));
        hb[2] = pk2(fmaf(bflo(xv[2]), g1[0], s1[0]), fmaf(bfhi(xv[2]), g1[1], s1[1]));
        hb[3] = pk2(fmaf(bflo(xv[3]), g1[2], s1[2]), fmaf(bfhi(xv[3]), g1[3], s1[3]));
        aq = mfma32(wq, hb, aq); ak = mfma32(wk, hb, ak); av = mfma32(hb, wv, av);
    }
    const float qs = 0.25f * 1.4426950408889634f;
#pragma unroll
    for (int s = 0; s < 2; ++s) {
        const int head = 2 * cp + s;
        u32x4 q = {pk2(aq[8 * s] * qs, aq[8 * s + 1] * qs), pk2(aq[8 * s + 2] * qs, aq[8 * s + 3] * qs),
                   pk2(aq[8 * s + 4] * qs, aq[8 * s + 5] * qs), pk2(aq[8 * s + 6] * qs, aq[8 * s + 7] * qs)};
        *(u32x4*)(ws + WS_Q + (size_t)(head * 64 + tt) * 1024 + lane * 16) = q;
        u32x4 k = {pk2(ak[8 * s], ak[8 * s + 1]), pk2(ak[8 * s + 2], ak[8 * s + 3]), pk2(ak[8 * s + 4], ak[8 * s + 5]), pk2(ak[8 * s + 6], ak[8 * s + 7])};
        *(u32x4*)(ws + WS_KV + (size_t)head * KVH + tt * KVT + lane * 16) = k;
        u32x4 v = {pk2(av[8 * s], av[8 * s + 1]), pk2(av[8 * s + 2], av[8 * s + 3]), pk2(av[8 * s + 4], av[8 * s + 5]), pk2(av[8 * s + 6], av[8 * s + 7])};
        const int vh = 2 * cp + ((lane >> 4) & 1);
        *(u32x4*)(ws + WS_KV + (size_t)vh * KVH + tt * KVT + 1024 + 512 * s + 256 * hh + 16 * (lane & 15)) = v;
    }
    if (lane < 4) {
        const u32x4 ones = {0x3F803F80u, 0x3F803F80u, 0x3F803F80u, 0x3F803F80u};
        *(u32x4*)(ws + WS_KV + (size_t)(2 * cp + (lane >> 1)) * KVH + tt * KVT + 2048 + 16 * (lane & 1)) = ones;
    }
}

constexpr int P2_Q = KVH, P2_X = KVH + 8192, P2_LDS = P2_X + 18432 + 64;
__global__ __launch_bounds__(1024) void p2_attn(const u16* __restrict__ wo, const u16* __restrict__ wg, const u16* __restrict__ wu,
                                                const u16* __restrict__ wd, char* __restrict__ ws) {
    extern __shared__ __attribute__((aligned(16))) char lds[];
    const int b = blockIdx.x, tid = threadIdx.x, lane = tid & 63, hh = lane >> 5;
    const int w = __builtin_amdgcn_readfirstlane(tid >> 6);
    const int head = 4 * (b & 7) + ((b >> 3) & 3), qb = b >> 5;
    const int qt = w & 7, kh = w >> 3;
    {
        const unsigned l0 = (unsigned)(uintptr_t)lds;
        const char* kv = ws + WS_KV + (size_t)head * KVH + lane * 16;
        if (w < 4) {
            const char* q = ws + WS_Q + (size_t)(head * 64 + qb * 8 + 2 * w) * 1024 + lane * 16;
            glds16(q, l0 + P2_Q + 2 * w * 1024); glds16(q + 1024, l0 + P2_Q + 2 * w * 1024 + 1024);
        } else {
            const int p = w - 4;
            glds16(kv + p * 1024, l0 + p * 1024); glds16(kv + (65 + p) * 1024, l0 + (65 + p) * 1024);
        }
#pragma unroll
        for (int i = 0; i < 7; ++i) {
            int j = 8 * i + qt; j = j > 52 ? 52 : j;
            const int p = 65 * kh + 12 + j;
            glds16(kv + p * 1024, l0 + p * 1024);
        }
    }
    WAITV_BAR(7);
    const u32x4 qf = lds_rd(lds, P2_Q + qt * 1024 + lane * 16);
    const int kbase = kh * 32 * KVT;
    const int koff = kbase + lane * 16;
    const bool dl = (lane & 16) == 0;
    const int voff0 = kbase + (dl ? 1024 + 256 * hh + 16 * (lane & 15) : 2048);
    const int voff1 = kbase + (dl ? 1536 + 256 * hh + 16 * (lane & 15) : 2048);
    float* xch = (float*)(lds + P2_X);
    volatile LAS int* flag = (volatile LAS int*)((LAS char*)lds + P2_X + 18432);
    float m;
    {
        const f32x16 z = {};
        const f32x16 s0 = mfma32(lds_rd(lds, lane * 16), qf, z);
        m = s0[0];
#pragma unroll
        for (int r = 1; r < 16; ++r) m = fmaxf(m, s0[r]);
        m = fmaxf(m, xhalf(m));
    }
    u32x4 pf0 = {}, pf1 = {}; int pdst0 = -1, pdst1 = -1;
    f32x16 o;
    for (int attempt = 0;; ++attempt) {
        f32x16 nm;
#pragma unroll
        for (int r = 0; r < 16; ++r) nm[r] = -m;
        o = f32x16{};
        f32x16 s = mfma32(lds_rd(lds, koff), qf, nm);
#pragma unroll 1
        for (int it = 0; it < 8; ++it) {
#pragma unroll
            for (int u = 0; u < 4; ++u) {
                const int t = 4 * it + u;
                const int tn = (t + 1) & 31;
                const u32x4 kn = lds_rd(lds, koff + tn * KVT);
                const u32x4 v0 = lds_rd(lds, voff0 + t * KVT), v1 = lds_rd(lds, voff1 + t * KVT);
                const f32x16 sn = mfma32(kn, qf, nm);
                u32x4 p0, p1;
#pragma unroll
                for (int j = 0; j < 4; ++j) p0[j] = pk2(__builtin_amdgcn_exp2f(s[2 * j]), __builtin_amdgcn_exp2f(s[2 * j + 1]));
                o = mfma32(v0, p0, o);
#pragma unroll
                for (int j = 0; j < 4; ++j) p1[j] = pk2(__builtin_amdgcn_exp2f(s[8 + 2 * j]), __builtin_amdgcn_exp2f(s[8 + 2 * j + 1]));
                o = mfma32(v1, p1, o);
                s = sn;
            }
            if (it == 0 && attempt == 0) {
                WAITV_BAR(0);
                const int gw = b * 16 + w;
                if (b < 224) {
                    const int nt = b >> 1, ks = (b & 1) * 16 + w, n = lane & 31;
                    const u16* src = (n < 16 ? wg : wu) + (size_t)(16 * ks + 4 * hh) * F_ + 16 * nt + (n & 15);
                    pf0 = gather_frag(src, F_); pdst0 = nt * 32 + ks;
                }
                if (gw < 512) {
                    const int nt = gw >> 5, ks = gw & 31;
                    pf1 = gather_frag(wo + (size_t)(16 * ks + 4 * hh) * D_ + 32 * nt + (lane & 31), D_); pdst1 = gw;
                } else if (gw >= 2304) {
                    const int f = gw - 2304, nt = f & 15, ks = f >> 4;
                    pf1 = gather_frag(wd + (size_t)(16 * ks + 4 * hh) * D_ + 32 * nt + (lane & 31), D_); pdst1 = 4096 + nt * 112 + ks;
                }
            }
        }
        if (tid == 0) *flag = 0;
        if (kh == 1) {
#pragma unroll
            for (int r = 0; r < 9; ++r) xch[(qt * 9 + r) * 64 + lane] = o[r];
        }
        BAR();
        if (kh == 0) {
#pragma unroll
            for (int r = 0; r < 9; ++r) o[r] += xch[(qt * 9 + r) * 64 + lane];
            bool bad = !(o[8] <= 1.0e12f);
#pragma unroll
            for (int r = 0; r < 8; ++r) bad |= !(fabsf(o[r]) <= 3.0e38f);
            if (attempt == 0 && __any(bad)) *flag = 1;
        }
        BAR();
        if (*flag == 0) break;
        {
            const f32x16 z = {};
            float mx = -INFINITY;
#pragma unroll 1
            for (int t = 0; t < 32; ++t) {
                const f32x16 s0 = mfma32(lds_rd(lds, koff + t * KVT), qf, z);
#pragma unroll
                for (int r = 0; r < 16; ++r) mx = fmaxf(mx, s0[r]);
            }
            mx = fmaxf(mx, xhalf(mx));
            BAR();
            xch[w * 64 + lane] = mx;
            BAR();
            m = fmaxf(mx, xch[(w ^ 8) * 64 + lane]);
            BAR();
        }
    }
    if (kh == 0) {
        const float rl = __builtin_amdgcn_rcpf(o[8]);
        u32x4 ov = {pk2(o[0] * rl, o[1] * rl), pk2(o[2] * rl, o[3] * rl), pk2(o[4] * rl, o[5] * rl), pk2(o[6] * rl, o[7] * rl)};
        *(u32x4*)(ws + WS_O + (size_t)((qb * 8 + qt) * 32 + head) * 1024 + lane * 16) = ov;
    }
    if (pdst1 >= 0) {
        char* dst = pdst1 >= 4096 ? ws + WS_WD + (size_t)(pdst1 - 4096) * 1024 : ws + WS_WO + (size_t)pdst1 * 1024;
        *(u32x4*)(dst + lane * 16) = pf1;
    }
    if (b < 224) {
        *(u32x4*)(ws + WS_WGU + (size_t)pdst0 * 1024 + lane * 16) = pf0;
        const float* mod = (const float*)(ws + WS_MOD);
        const int ks = pdst0 & 31, col = lane & 31;
        u32x4 bf = {0u, 0u, 0u, 0u};
        if (col < 2) {
            const float* src = mod + (col == 0 ? 2048 : 1536) + 16 * ks + 4 * hh;
            f32x4 a0 = *(const f32x4*)src, a1 = *(const f32x4*)(src + 8);
            if (col == 0) { a0 += 1.f; a1 += 1.f; }
            bf[0] = pk2(a0[0], a0[1]); bf[1] = pk2(a0[2], a0[3]); bf[2] = pk2(a1[0], a1[1]); bf[3] = pk2(a1[2], a1[3]);
        }
        const f32x16 z = {};
        const f32x16 cc = mfma32(pf0, bf, z);
        if (col < 2) {
#pragma unroll
            for (int r = 0; r < 16; ++r) xch[(w * 2 + col) * 32 + (r & 3) + 8 * (r >> 2) + 4 * hh] = cc[r];
        }
        BAR();
        if (tid < 64) {
            float s = 0.f;
#pragma unroll
            for (int i = 0; i < 16; ++i) s += xch[i * 64 + tid];
            ((float*)(ws + WS_C12))[((b & 1) * 112 + (b >> 1)) * 64 + tid] = s;
        }
    }
}

__device__ __forceinline__ void reduce4(f32x16 (&acc)[4], char* lds, int w, int lane) {
#pragma unroll
    for (int i = 1; i < 4; ++i)
#pragma unroll
        for (int q = 0; q < 4; ++q) {
            f32x4 v = {acc[i][4 * q], acc[i][4 * q + 1], acc[i][4 * q + 2], acc[i][4 * q + 3]};
            *(LAS f32x4*)((LAS char*)lds + (((w ^ i) * 3 + (i - 1)) * 4 + q) * 1024 + lane * 16) = v;
        }
    BAR();
#pragma unroll
    for (int i = 1; i < 4; ++i)
#pragma unroll
        for (int q = 0; q < 4; ++q) {
            const f32x4 v = *(const LAS f32x4*)((const LAS char*)lds + ((w * 3 + (i - 1)) * 4 + q) * 1024 + lane * 16);
            acc[0][4 * q] += v[0]; acc[0][4 * q + 1] += v[1]; acc[0][4 * q + 2] += v[2]; acc[0][4 * q + 3] += v[3];
        }
}

__global__ __launch_bounds__(256) void p3_wo(const u16* __restrict__ x, char* __restrict__ ws) {
    __shared__ __attribute__((aligned(16))) char lds[49152];
    const int b = blockIdx.x, tid = threadIdx.x, lane = tid & 63, hh = lane >> 5;
    const int w = __builtin_amdgcn_readfirstlane(tid >> 6);
    const int i5 = b >> 3;
    const int tt = (b & 7) * 8 + (i5 >> 3) * 2 + (w & 1), nt = (i5 & 7) * 2 + (w >> 1);
    const char* a0p = ws + WS_WO + (size_t)(nt * 32 + 8 * w) * 1024 + lane * 16;
    const char* a1p = ws + WS_WO + (size_t)((nt ^ 1) * 32 + 8 * w) * 1024 + lane * 16;
    const char* b0p = ws + WS_O + (size_t)(tt * 32 + 8 * w) * 1024 + lane * 16;
    const char* b1p = ws + WS_O + (size_t)((tt ^ 1) * 32 + 8 * w) * 1024 + lane * 16;
    u32x4 a0[8], a1[8], b0[8], b1[8];
#pragma unroll
    for (int k = 0; k < 8; ++k) {
        b0[k] = *(const u32x4*)(b0p + k * 1024); a0[k] = *(const u32x4*)(a0p + k * 1024);
        b1[k] = *(const u32x4*)(b1p + k * 1024); a1[k] = *(const u32x4*)(a1p + k * 1024);
    }
    const int tok = 32 * tt + (lane & 31);
    const float* mod = (const float*)(ws + WS_MOD);
    u32x2 xr[4]; f32x4 ga[4], cb[4];
#pragma unroll
    for (int q = 0; q < 4; ++q) {
        const int n = 32 * nt + 8 * q + 4 * hh;
        xr[q] = *(const u32x2*)(x + (size_t)tok * D_ + n);
        ga[q] = *(const f32x4*)(mod + 1024 + n); cb[q] = *(const f32x4*)(mod + 2048 + n);
    }
    f32x16 acc[4] = {};
#pragma unroll
    for (int k = 0; k < 8; ++k) {
        acc[0] = mfma32(a0[k], b0[k], acc[0]); acc[1] = mfma32(a0[k], b1[k], acc[1]);
        acc[2] = mfma32(a1[k], b0[k], acc[2]); acc[3] = mfma32(a1[k], b1[k], acc[3]);
    }
    reduce4(acc, lds, w, lane);
    float x1[16]; unsigned x1p[8], yp[8];
#pragma unroll
    for (int q = 0; q < 4; ++q) {
        x1p[2 * q] = pk2(fmaf(ga[q][0], acc[0][4 * q], bflo(xr[q][0])), fmaf(ga[q][1], acc[0][4 * q + 1], bfhi(xr[q][0])));
        x1p[2 * q + 1] = pk2(fmaf(ga[q][2], acc[0][4 * q + 2], bflo(xr[q][1])), fmaf(ga[q][3], acc[0][4 * q + 3], bfhi(xr[q][1])));
        x1[4 * q] = bflo(x1p[2 * q]); x1[4 * q + 1] = bfhi(x1p[2 * q]); x1[4 * q + 2] = bflo(x1p[2 * q + 1]); x1[4 * q + 3] = bfhi(x1p[2 * q + 1]);
        yp[2 * q] = pk2(x1[4 * q] * rbf(1.f + cb[q][0]), x1[4 * q + 1] * rbf(1.f + cb[q][1]));
        yp[2 * q + 1] = pk2(x1[4 * q + 2] * rbf(1.f + cb[q][2]), x1[4 * q + 3] * rbf(1.f + cb[q][3]));
    }
#pragma unroll
    for (int s = 0; s < 2; ++s) {
        const size_t off = (size_t)(tt * 32 + 2 * nt + s) * 1024 + lane * 16;
        *(u32x4*)(ws + WS_X1 + off) = u32x4{x1p[4 * s], x1p[4 * s + 1], x1p[4 * s + 2], x1p[4 * s + 3]};
        *(u32x4*)(ws + WS_Y + off) = u32x4{yp[4 * s], yp[4 * s + 1], yp[4 * s + 2], yp[4 * s + 3]};
    }
    float s = 0.f;
#pragma unroll
    for (int r = 0; r < 16; ++r) s += x1[r];
    s += xhalf(s);
    const float mean = s * (1.f / 32.f);
    float q2 = 0.f;
#pragma unroll
    for (int r = 0; r < 16; ++r) { const float d = x1[r] - mean; q2 += d * d; }
    q2 += xhalf(q2);
    if (hh == 0) *(f32x2*)(ws + WS_STAT + (size_t)(tok * 16 + nt) * 8) = f32x2{mean, q2};
}

constexpr int P4_LDS = 131072 + 1024;
__global__ __launch_bounds__(512) void p4_gu(char* __restrict__ ws) {
    extern __shared__ __attribute__((aligned(16))) char lds[];
    const int b = blockIdx.x, tid = threadIdx.x, lane = tid & 63, hh = lane >> 5;
    const int w = __builtin_amdgcn_readfirstlane(tid >> 6);
    const int i5 = b >> 3, rp = (b & 7) * 2 + (i5 >> 4), cg = i5 & 15;
    if (w == 7) {
        const unsigned l0 = (unsigned)(uintptr_t)lds;
        const char* src = ws + WS_Y + (size_t)rp * 4 * 32768 + lane * 16;
#pragma unroll 1
        for (int st = 0; st < 4; ++st) {
#pragma unroll
            for (int i = 0; i < 32; ++i) {
                const int ks = st * 8 + (i >> 2), t = i & 3;
                glds16(src + t * 32768 + ks * 1024, l0 + (ks * 4 + t) * 1024);
            }
            if (st > 0) WAITV_BAR(32);
        }
        WAITV_BAR(0);
        return;
    }
    const int nt = cg * 7 + w;
    f32x4 st4[4];
    if (w < 4) {
        const char* sp = ws + WS_STAT + (size_t)(128 * rp + 32 * w + (lane & 31)) * 128 + hh * 64;
#pragma unroll
        for (int i = 0; i < 4; ++i) st4[i] = *(const f32x4*)(sp + 16 * i);
    }
    const char* wp = ws + WS_WGU + (size_t)nt * 32768 + lane * 16;
    u32x4 wf[32];
#pragma unroll
    for (int ks = 0; ks < 32; ++ks) wf[ks] = *(const u32x4*)(wp + ks * 1024);
    if (w < 4) {
        float s = 0.f;
#pragma unroll
        for (int i = 0; i < 4; ++i) s += st4[i][0] + st4[i][2];
        s += xhalf(s);
        const float mean = s * (1.f / 16.f);
        float q = 0.f;
#pragma unroll
        for (int i = 0; i < 4; ++i) {
            const float d0 = st4[i][0] - mean, d1 = st4[i][2] - mean;
            q += st4[i][1] + st4[i][3] + 32.f * (d0 * d0 + d1 * d1);
        }
        q += xhalf(q);
        const float rs = __builtin_amdgcn_rsqf(q * (1.f / 512.f) + 1e-5f);
        if (hh == 0) *(LAS f32x2*)((LAS char*)lds + 131072 + (32 * w + lane) * 8) = f32x2{rs, -rs * mean};
    }
    f32x16 acc[4] = {};
#pragma unroll
    for (int ks = 0; ks < 32; ++ks) {
        if ((ks & 7) == 0) BAR();
#pragma unroll
        for (int t = 0; t < 4; ++t) acc[t] = mfma32(wf[ks], lds_rd(lds, (ks * 4 + t) * 1024 + lane * 16), acc[t]);
    }
    const float* c12 = (const float*)(ws + WS_C12) + nt * 64 + 4 * hh;
    f32x4 c1[4], c2[4];
#pragma unroll
    for (int q = 0; q < 4; ++q) {
        c1[q] = *(const f32x4*)(c12 + 8 * q) + *(const f32x4*)(c12 + 112 * 64 + 8 * q);
        c2[q] = *(const f32x4*)(c12 + 32 + 8 * q) + *(const f32x4*)(c12 + 112 * 64 + 32 + 8 * q);
    }
#pragma unroll
    for (int t = 0; t < 4; ++t) {
        const f32x2 rr = *(const LAS f32x2*)((const LAS char*)lds + 131072 + (32 * t + (lane & 31)) * 8);
        float a[8];
#pragma unroll
        for (int r = 0; r < 8; ++r) {
            const float g = fmaf(acc[t][r], rr[0], fmaf(rr[1], c1[r >> 2][r & 3], c2[r >> 2][r & 3]));
            const float u = fmaf(acc[t][8 + r], rr[0], fmaf(rr[1], c1[2 + (r >> 2)][r & 3], c2[2 + (r >> 2)][r & 3]));
            const float e = __builtin_amdgcn_exp2f(-1.4426950408889634f * g);
            a[r] = g * u * __builtin_amdgcn_rcpf(1.f + e);
        }
        *(u32x4*)(ws + WS_ACT + (size_t)((4 * rp + t) * 112 + nt) * 1024 + lane * 16) =
            u32x4{pk2(a[0], a[1]), pk2(a[2], a[3]), pk2(a[4], a[5]), pk2(a[6], a[7])};
    }
}

__global__ __launch_bounds__(256) void p5_down(char* __restrict__ ws, u16* __restrict__ out) {
    __shared__ __attribute__((aligned(16))) char lds[49152];
    const int b = blockIdx.x, tid = threadIdx.x, lane = tid & 63, hh = lane >> 5;
    const int w = __builtin_amdgcn_readfirstlane(tid >> 6);
    const int i5 = b >> 3;
    const int tt = (b & 7) * 8 + (i5 >> 3) * 2 + (w & 1), nt = (i5 & 7) * 2 + (w >> 1);
    const char* a0p = ws + WS_WD + (size_t)(nt * 112 + 28 * w) * 1024 + lane * 16;
    const char* a1p = ws + WS_WD + (size_t)((nt ^ 1) * 112 + 28 * w) * 1024 + lane * 16;
    const char* b0p = ws + WS_ACT + (size_t)(tt * 112 + 28 * w) * 1024 + lane * 16;
    const char* b1p = ws + WS_ACT + (size_t)((tt ^ 1) * 112 + 28 * w) * 1024 + lane * 16;
    constexpr int WIN = 12;
    u32x4 a0[28], a1[28], b0[28], b1[28];
#pragma unroll
    for (int k = 0; k < WIN; ++k) {
        b0[k] = *(const u32x4*)(b0p + k * 1024); a0[k] = *(const u32x4*)(a0p + k * 1024);
        b1[k] = *(const u32x4*)(b1p + k * 1024); a1[k] = *(const u32x4*)(a1p + k * 1024);
    }
    __builtin_amdgcn_sched_barrier(0);
    f32x16 acc[4] = {};
#pragma unroll
    for (int k = 0; k < 28; ++k) {
        acc[0] = mfma32(a0[k], b0[k], acc[0]); acc[1] = mfma32(a0[k], b1[k], acc[1]);
        acc[2] = mfma32(a1[k], b0[k], acc[2]); acc[3] = mfma32(a1[k], b1[k], acc[3]);
        if (k + WIN < 28) {
            const int n = k + WIN;
            b0[n] = *(const u32x4*)(b0p + n * 1024); a0[n] = *(const u32x4*)(a0p + n * 1024);
            b1[n] = *(const u32x4*)(b1p + n * 1024); a1[n] = *(const u32x4*)(a1p + n * 1024);
        }
        __builtin_amdgcn_sched_barrier(0);
    }
    const float* mod = (const float*)(ws + WS_MOD);
    u32x4 xr[2]; f32x4 gb[4];
#pragma unroll
    for (int s = 0; s < 2; ++s) xr[s] = *(const u32x4*)(ws + WS_X1 + (size_t)(tt * 32 + 2 * nt + s) * 1024 + lane * 16);
#pragma unroll
    for (int q = 0; q < 4; ++q) gb[q] = *(const f32x4*)(mod + 2560 + 32 * nt + 8 * q + 4 * hh);
    reduce4(acc, lds, w, lane);
    u16* orow = out + (size_t)(32 * tt + (lane & 31)) * D_ + 32 * nt + 4 * hh;
#pragma unroll
    for (int q = 0; q < 4; ++q) {
        const unsigned x0 = xr[q >> 1][2 * (q & 1)], x1 = xr[q >> 1][2 * (q & 1) + 1];
        u32x2 o = {pk2(fmaf(gb[q][0], acc[0][4 * q], bflo(x0)), fmaf(gb[q][1], acc[0][4 * q + 1], bfhi(x0))),
                   pk2(fmaf(gb[q][2], acc[0][4 * q + 2], bflo(x1)), fmaf(gb[q][3], acc[0][4 * q + 3], bfhi(x1)))};
        *(u32x2*)(orow + 8 * q) = o;
    }
}

extern "C" void kernel_launch(void* const* d_in, const int* in_sizes, int n_in, void* d_out, int out_size, void* d_ws, size_t ws_size,
                              hipStream_t stream) {
    const u16* x = (const u16*)d_in[0];
    const u16* wqkv = (const u16*)d_in[1];
    const u16* wo = (const u16*)d_in[2];
    const u16* wg = (const u16*)d_in[4];
    const u16* wu = (const u16*)d_in[5];
    const u16* wd = (const u16*)d_in[6];
    const u16* c = (const u16*)d_in[7];
    const u16* wmod = (const u16*)d_in[8];
    char* ws = (char*)d_ws;
    (void)hipFuncSetAttribute((const void*)p1_qkv, hipFuncAttributeMaxDynamicSharedMemorySize, P1_LDS);
    (void)hipFuncSetAttribute((const void*)p2_attn, hipFuncAttributeMaxDynamicSharedMemorySize, P2_LDS);
    (void)hipFuncSetAttribute((const void*)p4_gu, hipFuncAttributeMaxDynamicSharedMemorySize, P4_LDS);
    k0_prep<<<256, 512, 0, stream>>>(x, wqkv, c, wmod, ws);
    p1_qkv<<<256, 512, P1_LDS, stream>>>(ws);
    p2_attn<<<256, 1024, P2_LDS, stream>>>(wo, wg, wu, wd, ws);
    p3_wo<<<256, 256, 0, stream>>>(x, ws);
    p4_gu<<<256, 512, P4_LDS, stream>>>(ws);
    p5_down<<<256, 256, 0, stream>>>(ws, (u16*)d_out);
}
```

```cpp
#include <hip/hip_runtime.h>
#include <hip/hip_bf16.h>
#include <stdint.h>

typedef __attribute__((ext_vector_type(8))) __bf16 bf16x8;
typedef __attribute__((ext_vector_type(2))) __bf16 bf16x2;
typedef __attribute__((ext_vector_type(2))) float f32x2;
typedef __attribute__((ext_vector_type(4))) float f32x4;
typedef __attribute__((ext_vector_type(16))) float f32x16;
typedef __attribute__((ext_vector_type(4))) unsigned u32x4;
typedef __attribute__((ext_vector_type(2))) unsigned u32x2;
typedef unsigned short u16;
#define LAS __attribute__((address_space(3)))
#define ST_DECL
#define ST(i)
#define ST_FLUSH(kid)

constexpr int S_ = 2048, D_ = 512, F_ = 1792;
constexpr int KVT = 2080;
constexpr int KVH = 64 * KVT;
constexpr size_t WS_MOD = 0;
constexpr size_t WS_C12 = 16384;
constexpr size_t WS_STAT = WS_C12 + 65536;
constexpr size_t WS_XH = WS_STAT + 262144;
constexpr size_t WS_WQKV = WS_XH + 2097152;
constexpr size_t WS_Q = WS_WQKV + 1572864;
constexpr size_t WS_KV = WS_Q + 2097152;
constexpr size_t WS_O = WS_KV + 4194304;
constexpr size_t WS_WO = WS_O + 2097152;
constexpr size_t WS_WGU = WS_WO + 524288;
constexpr size_t WS_WD = WS_WGU + 3670016;
constexpr size_t WS_X1 = WS_WD + 1835008;
constexpr size_t WS_Y = WS_X1 + 2097152;
constexpr size_t WS_ACT = WS_Y + 2097152;

__device__ __forceinline__ float bflo(unsigned u) { return __uint_as_float(u << 16); }
__device__ __forceinline__ float bfhi(unsigned u) { return __uint_as_float(u & 0xffff0000u); }
__device__ __forceinline__ unsigned pk2(float a, float b) {
    f32x2 f = {a, b};
    return __builtin_bit_cast(unsigned, __builtin_convertvector(f, bf16x2));
}
__device__ __forceinline__ float rbf(float a) { return (float)(__bf16)a; }
__device__ __forceinline__ f32x16 mfma32(u32x4 a, u32x4 b, f32x16 c) {
    return __builtin_amdgcn_mfma_f32_32x32x16_bf16(__builtin_bit_cast(bf16x8, a), __builtin_bit_cast(bf16x8, b), c, 0, 0, 0);
}
__device__ __forceinline__ float xhalf(float v) {
    auto r = __builtin_amdgcn_permlane32_swap(__float_as_uint(v), __float_as_uint(v), false, false);
    return (threadIdx.x & 32) ? __uint_as_float(r[0]) : __uint_as_float(r[1]);
}
__device__ __forceinline__ void glds16(const char* g, unsigned voff, unsigned lds_base) {
    unsigned sv;
    asm volatile("s_mov_b32 %0, m0\n\ts_mov_b32 m0, %3\n\ts_nop 0\n\tglobal_load_lds_dwordx4 %1, %2\n\ts_mov_b32 m0, %0"
                 : "=&s"(sv) : "v"(voff), "s"(g), "s"(lds_base) : "memory");
}
constexpr unsigned WS_END = WS_ACT + 64 * 112 * 1024;
typedef __amdgpu_buffer_rsrc_t rsrc_t;
__device__ __forceinline__ rsrc_t ws_rsrc(char* ws) { return __builtin_amdgcn_make_buffer_rsrc(ws, 0, WS_END, 0x00020000); }
__device__ __forceinline__ void st16(rsrc_t r, unsigned off, u32x4 v) { __builtin_amdgcn_raw_buffer_store_b128(v, r, off, 0, 16); }
__device__ __forceinline__ void st8(rsrc_t r, unsigned off, u32x2 v) { __builtin_amdgcn_raw_buffer_store_b64(v, r, off, 0, 16); }
__device__ __forceinline__ void st4(rsrc_t r, unsigned off, unsigned v) { __builtin_amdgcn_raw_buffer_store_b32(v, r, off, 0, 16); }
__device__ __forceinline__ rsrc_t mk_rsrc(const char* p, unsigned bytes) { return __builtin_amdgcn_make_buffer_rsrc((void*)p, 0, bytes, 0x00020000); }
__device__ __forceinline__ u32x4 ld16(rsrc_t r, unsigned voff, unsigned soff) { return __builtin_amdgcn_raw_buffer_load_b128(r, voff, soff, 0); }
#define SB() __builtin_amdgcn_sched_barrier(0)
#define PIN(x) asm volatile("" : "+v"(x))
#define BAR() asm volatile("s_waitcnt lgkmcnt(0)\n\ts_barrier" ::: "memory")
#define BARE() asm volatile("s_barrier" ::: "memory")
#define WAITV_BAR(N) asm volatile("s_waitcnt vmcnt(" #N ") lgkmcnt(0)\n\ts_barrier" ::: "memory")
__device__ __forceinline__ u32x4 lds_rd(const char* base, int off) { return *(const LAS u32x4*)((const LAS char*)base + off); }

__device__ __forceinline__ u32x4 gather_frag(const u16* p, int ld) {
    u32x4 r;
    r[0] = (unsigned)p[0] | ((unsigned)p[ld] << 16);
    r[1] = (unsigned)p[2 * ld] | ((unsigned)p[3 * ld] << 16);
    r[2] = (unsigned)p[8 * ld] | ((unsigned)p[9 * ld] << 16);
    r[3] = (unsigned)p[10 * ld] | ((unsigned)p[11 * ld] << 16);
    return r;
}

__global__ __launch_bounds__(512) void k0_prep(const u16* __restrict__ x, const u16* __restrict__ wqkv, const u16* __restrict__ c,
                                               const u16* __restrict__ wmod, char* __restrict__ ws) {
    __shared__ float red[8][32];
    ST_DECL; ST(0);
    const int b = blockIdx.x, tid = threadIdx.x, lane = tid & 63, w = tid >> 6;
    if (b < 96) {
        const int ch = lane & 3, r = lane >> 2;
        u32x4 wv[4]; float sc[4];
#pragma unroll
        for (int i = 0; i < 4; ++i) {
            const int row = 64 * w + 16 * i + r;
            wv[i] = *(const u32x4*)(wmod + (size_t)row * 3072 + 32 * b + 8 * ch);
            sc[i] = bflo(c[row]);
        }
        float acc[8];
#pragma unroll
        for (int j = 0; j < 8; ++j) acc[j] = 0.f;
#pragma unroll
        for (int i = 0; i < 4; ++i) {
            const float s = rbf(sc[i] / (1.f + __expf(-sc[i])));
#pragma unroll
            for (int j = 0; j < 4; ++j) { acc[2 * j] += s * bflo(wv[i][j]); acc[2 * j + 1] += s * bfhi(wv[i][j]); }
        }
#pragma unroll
        for (int j = 0; j < 8; ++j) {
            acc[j] += __shfl_xor(acc[j], 4); acc[j] += __shfl_xor(acc[j], 8); acc[j] += __shfl_xor(acc[j], 16); acc[j] += __shfl_xor(acc[j], 32);
        }
        if (lane < 4) {
#pragma unroll
            for (int j = 0; j < 8; ++j) red[w][8 * ch + j] = acc[j];
        }
        __syncthreads();
        if (tid < 32) {
            float s = 0.f;
#pragma unroll
            for (int i = 0; i < 8; ++i) s += red[i][tid];
            st4(ws_rsrc(ws), WS_MOD + (32 * b + tid) * 4, __float_as_uint(rbf(s)));
        }
    } else if (b < 160) {
        const int tt = b - 96, r = lane & 3, cc = lane >> 2, tok = 4 * w + r;
        const u16* xr = x + (size_t)(32 * tt + tok) * D_;
        u32x4 v[4];
        v[0] = *(const u32x4*)(xr + 16 * cc); v[1] = *(const u32x4*)(xr + 16 * cc + 8);
        v[2] = *(const u32x4*)(xr + 16 * cc + 256); v[3] = *(const u32x4*)(xr + 16 * cc + 264);
        float f[32];
#pragma unroll
        for (int i = 0; i < 4; ++i)
#pragma unroll
            for (int j = 0; j < 4; ++j) { f[8 * i + 2 * j] = bflo(v[i][j]); f[8 * i + 2 * j + 1] = bfhi(v[i][j]); }
        float s = 0.f;
#pragma unroll
        for (int i = 0; i < 32; ++i) s += f[i];
        s += __shfl_xor(s, 4); s += __shfl_xor(s, 8); s += __shfl_xor(s, 16); s += __shfl_xor(s, 32);
        const float mean = s * (1.f / 512.f);
        float q = 0.f;
#pragma unroll
        for (int i = 0; i < 32; ++i) { f[i] -= mean; q += f[i] * f[i]; }
        q += __shfl_xor(q, 4); q += __shfl_xor(q, 8); q += __shfl_xor(q, 16); q += __shfl_xor(q, 32);
        const float rs = __builtin_amdgcn_rsqf(q * (1.f / 512.f) + 1e-5f);
        const unsigned dst = WS_XH + tt * 32768 + tok * 16;
        const rsrc_t rs_ = ws_rsrc(ws);
#pragma unroll
        for (int p = 0; p < 2; ++p) {
            const float* g = f + 16 * p;
            u32x4 h0 = {pk2(g[0] * rs, g[1] * rs), pk2(g[2] * rs, g[3] * rs), pk2(g[8] * rs, g[9] * rs), pk2(g[10] * rs, g[11] * rs)};
            u32x4 h1 = {pk2(g[4] * rs, g[5] * rs), pk2(g[6] * rs, g[7] * rs), pk2(g[12] * rs, g[13] * rs), pk2(g[14] * rs, g[15] * rs)};
            st16(rs_, dst + (cc + 16 * p) * 1024, h0);
            st16(rs_, dst + (cc + 16 * p) * 1024 + 512, h1);
        }
    } else {
        const int f0 = (b - 160) * 16 + 2 * w;
        u32x4 fr[2];
#pragma unroll
        for (int i = 0; i < 2; ++i) {
            const int f = f0 + i, nt = f >> 5, ks = f & 31;
            fr[i] = gather_frag(wqkv + (size_t)(16 * ks + 4 * (lane >> 5)) * 1536 + 32 * nt + (lane & 31), 1536);
        }
#pragma unroll
        for (int i = 0; i < 2; ++i) st16(ws_rsrc(ws), WS_WQKV + (f0 + i) * 1024 + lane * 16, fr[i]);
    }
    ST(7); ST_FLUSH(0);
}

constexpr int P1_W = 4608, P1_LDS = P1_W + 98304 + 3072;
__global__ __launch_bounds__(512) void p1_qkv(char* __restrict__ ws) {
    extern __shared__ __attribute__((aligned(16))) char lds[];
    const int b = blockIdx.x, tid = threadIdx.x, lane = tid & 63;
    const int w = __builtin_amdgcn_readfirstlane(tid >> 6);
    const int i5 = b >> 3, rp = 4 * ((b & 7) >> 1) + (i5 >> 3), cp = 8 * (b & 1) + (i5 & 7);
    ST_DECL; ST(0);
    if (w < 4) {
        const unsigned l0 = (unsigned)(uintptr_t)lds + P1_W;
        const char* src = ws + WS_WQKV + (size_t)cp * 32768;
#pragma unroll
        for (int i = 0; i < 24; ++i) {
            const int p = 4 * i + w, ks = p / 3, wh = p - 3 * ks;
            glds16(src + wh * (16 * 32768) + ks * 1024, lane * 16, l0 + p * 1024);
            if (i == 11 || i == 17 || i == 23) WAITV_BAR(6);
        }
        WAITV_BAR(0);
        ST(7); ST_FLUSH(1);
        return;
    }
    const float* mod = (const float*)(ws + WS_MOD);
    const int t2 = 2 * (tid - 256);
    const f32x2 sa2 = *(const f32x2*)(mod + t2), ca2 = *(const f32x2*)(mod + 512 + t2);
    const int tt = 4 * rp + w - 4;
    const rsrc_t xs = mk_rsrc(ws + WS_XH + (size_t)tt * 32768, 32768);
    u32x4 xf[16];
#pragma unroll
    for (int ks = 0; ks < 16; ++ks) xf[ks] = ld16(xs, lane * 16 + (ks & 3) * 1024, (ks >> 2) * 4096);
    SB();
    {
        const int ks = t2 >> 4, hh = (t2 >> 2) & 1, j = ((t2 >> 3) & 1) * 4 + (t2 & 3);
        float* tab = (float*)lds + (ks * 2 + hh) * 16 + j;
        f32x2 g2 = {rbf(1.f + ca2[0]), rbf(1.f + ca2[1])};
        *(f32x2*)tab = g2; *(f32x2*)(tab + 8) = sa2;
    }
    f32x16 aq = {}, ak = {}, av = {};
    const int hh = lane >> 5;
    const LAS char* tb = (const LAS char*)lds + hh * 64;
    const LAS char* wb = (const LAS char*)lds + P1_W + lane * 16;
    ST(1);
    BAR();
    ST(2);
#define P1_GS(i, o) do { g0[i] = *(const LAS f32x4*)(tb + (o)); s0[i] = *(const LAS f32x4*)(tb + (o) + 32); g1[i] = *(const LAS f32x4*)(tb + (o) + 16); s1[i] = *(const LAS f32x4*)(tb + (o) + 48); } while (0)
#define P1_H0(H, X, i) do { H[0] = pk2(fmaf(bflo(X[0]), g0[i][0], s0[i][0]), fmaf(bfhi(X[0]), g0[i][1], s0[i][1])); H[1] = pk2(fmaf(bflo(X[1]), g0[i][2], s0[i][2]), fmaf(bfhi(X[1]), g0[i][3], s0[i][3])); } while (0)
#define P1_H1(H, X, i) do { H[2] = pk2(fmaf(bflo(X[2]), g1[i][0], s1[i][0]), fmaf(bfhi(X[2]), g1[i][1], s1[i][1])); H[3] = pk2(fmaf(bflo(X[3]), g1[i][2], s1[i][2]), fmaf(bfhi(X[3]), g1[i][3], s1[i][3])); } while (0)
    f32x4 g0[2], g1[2], s0[2], s1[2]; u32x4 wq, wk, wv, hb[2];
    P1_GS(0, 0); P1_GS(1, 128);
    wq = *(const LAS u32x4*)(wb); wk = *(const LAS u32x4*)(wb + 1024); wv = *(const LAS u32x4*)(wb + 2048);
    P1_H0(hb[0], xf[0], 0); P1_H1(hb[0], xf[0], 0);
    SB();
#pragma unroll 1
    for (int it = 0; it < 2; ++it) {
#pragma unroll
        for (int u = 0; u < 16; ++u) {
            if (u == 7 || (u == 15 && it == 0)) BARE();
            const int c = u & 1, n = c ^ 1;
            const u32x4 X = xf[(u + 1) & 15];
            float e0, e1;
#define P1_E(i, G, S, j) e0 = fmaf(bflo(X[i]), G[n][j], S[n][j]); e1 = fmaf(bfhi(X[i]), G[n][j + 1], S[n][j + 1]); hb[n][i] = pk2(e0, e1)
            aq = mfma32(wq, hb[c], aq); wq = *(const LAS u32x4*)(wb + (u + 1) * 3072); SB();
            P1_E(0, g0, s0, 0); g0[c] = *(const LAS f32x4*)(tb + (u + 2) * 128); SB();
            xf[u] = ld16(xs, lane * 16 + (u & 3) * 1024, it * 16384 + 16384 + (u >> 2) * 4096); s0[c] = *(const LAS f32x4*)(tb + (u + 2) * 128 + 32); SB();
            ak = mfma32(wk, hb[c], ak); SB();
            P1_E(1, g0, s0, 2); wk = *(const LAS u32x4*)(wb + (u + 1) * 3072 + 1024); SB();
            P1_E(2, g1, s1, 0); g1[c] = *(const LAS f32x4*)(tb + (u + 2) * 128 + 16); SB();
            av = mfma32(hb[c], wv, av); wv = *(const LAS u32x4*)(wb + (u + 1) * 3072 + 2048); SB();
            P1_E(3, g1, s1, 2); PIN(hb[n]); s1[c] = *(const LAS f32x4*)(tb + (u + 2) * 128 + 48); SB();
        }
        wb += 16 * 3072; tb += 16 * 128;
    }
    ST(5);
    const float qs = 0.25f * 1.4426950408889634f;
    const rsrc_t rs = ws_rsrc(ws);
#pragma unroll
    for (int s = 0; s < 2; ++s) {
        const int head = 2 * cp + s;
        u32x4 k = {pk2(ak[8 * s], ak[8 * s + 1]), pk2(ak[8 * s + 2], ak[8 * s + 3]), pk2(ak[8 * s + 4], ak[8 * s + 5]), pk2(ak[8 * s + 6], ak[8 * s + 7])};
        st16(rs, WS_KV + (head * 64 + tt) * 2048 + lane * 16, k);
        u32x4 q = {pk2(aq[8 * s] * qs, aq[8 * s + 1] * qs), pk2(aq[8 * s + 2] * qs, aq[8 * s + 3] * qs),
                   pk2(aq[8 * s + 4] * qs, aq[8 * s + 5] * qs), pk2(aq[8 * s + 6] * qs, aq[8 * s + 7] * qs)};
        st16(rs, WS_Q + (head * 64 + tt) * 1024 + lane * 16, q);
        u32x4 v = {pk2(av[8 * s], av[8 * s + 1]), pk2(av[8 * s + 2], av[8 * s + 3]), pk2(av[8 * s + 4], av[8 * s + 5]), pk2(av[8 * s + 6], av[8 * s + 7])};
        const int vh = 2 * cp + ((lane >> 4) & 1);
        st16(rs, WS_KV + (vh * 64 + tt) * 2048 + 1024 + 512 * s + 256 * hh + 16 * (lane & 15), v);
    }
    ST(7); ST_FLUSH(1);
}

typedef __attribute__((ext_vector_type(8))) unsigned u32x8;
template <int LIM> __device__ __forceinline__ u32x8 gather_async(const u16* p, int ld, int v) {
    const u16* p2 = p + 2 * ld; const u16* p8 = p + 8 * ld; const u16* p10 = p + 10 * ld;
    const u16* p1 = p + ld; const u16* p3 = p2 + ld; const u16* p9 = p8 + ld; const u16* p11 = p10 + ld;
    unsigned a, b, c, d, e, f, g, h;
    asm volatile("s_cmp_ge_u32 %16, %17\n\ts_cbranch_scc1 .Lgskip%=\n\t"
                 "global_load_ushort %0, %8, off\n\tglobal_load_ushort %1, %9, off\n\tglobal_load_ushort %2, %10, off\n\tglobal_load_ushort %3, %11, off\n\t"
                 "global_load_ushort %4, %12, off\n\tglobal_load_ushort %5, %13, off\n\tglobal_load_ushort %6, %14, off\n\tglobal_load_ushort %7, %15, off\n"
                 ".Lgskip%=:"
                 : "=&v"(a), "=&v"(b), "=&v"(c), "=&v"(d), "=&v"(e), "=&v"(f), "=&v"(g), "=&v"(h)
                 : "v"(p), "v"(p1), "v"(p2), "v"(p3), "v"(p8), "v"(p9), "v"(p10), "v"(p11), "s"(v), "i"(LIM) : "memory", "scc");
    return u32x8{a, b, c, d, e, f, g, h};
}
__device__ __forceinline__ u32x4 pack8(u32x8 g) { return u32x4{g[0] | (g[1] << 16), g[2] | (g[3] << 16), g[4] | (g[5] << 16), g[6] | (g[7] << 16)}; }
constexpr int P2_Q = KVH, P2_X = KVH + 8192, P2_C = P2_Q  , P2_LDS = P2_X + 18432;
#define EX(x) __builtin_amdgcn_exp2f(x)
__global__ __launch_bounds__(1024) void p2_attn(const u16* __restrict__ wo, const u16* __restrict__ wg, const u16* __restrict__ wu,
                                                const u16* __restrict__ wd, char* __restrict__ ws) {
    extern __shared__ __attribute__((aligned(16))) char lds[];
    const int b = blockIdx.x, tid = threadIdx.x, lane = tid & 63, hh = lane >> 5;
    const int w = __builtin_amdgcn_readfirstlane(tid >> 6);
    const int head = 4 * (b & 7) + ((b >> 3) & 3), qb = b >> 5;
    const int qt = w & 7, kh = w >> 3;
    ST_DECL; ST(0);
    const unsigned l0 = (unsigned)(uintptr_t)lds;
    const char* kv = ws + WS_KV + (size_t)head * 131072;
    const unsigned l16 = lane * 16;
#define DMA_P(p) glds16(kv + (p) * 1024, l16, l0 + ((p) >> 1) * KVT + ((p) & 1) * 1024)
    if (w < 4) {
        const char* q = ws + WS_Q + (size_t)(head * 64 + qb * 8 + 2 * w) * 1024;
        glds16(q, l16, l0 + P2_Q + 2 * w * 1024); glds16(q + 1024, l16, l0 + P2_Q + 2 * w * 1024 + 1024);
        if (w == 0) *(LAS u32x4*)((LAS char*)lds + lane * KVT + 2048) = u32x4{0x3F803F80u, 0x3F803F80u, 0x3F803F80u, 0x3F803F80u};
        if (w == 1) *(LAS u32x4*)((LAS char*)lds + lane * KVT + 2064) = u32x4{0u, 0u, 0u, 0u};
    } else {
        DMA_P(w - 4); DMA_P(60 + w);
    }
#define DMA_B(i) do { int j_ = 8 * (i) + qt; j_ = j_ > 51 ? 51 : j_; const int p_ = 64 * kh + 12 + j_; DMA_P(p_); } while (0)
    ST(1);
    WAITV_BAR(0);
    ST(2);
    DMA_B(0); DMA_B(1);
    const u32x4 qf = lds_rd(lds, P2_Q + qt * 1024 + lane * 16);
    const LAS char* kbase = (const LAS char*)lds + kh * 32 * KVT;
    const bool dl = (lane & 16) == 0;
    float* xch = (float*)(lds + P2_X);
    volatile LAS int* flag = (volatile LAS int*)((LAS char*)lds + P2_C + 4096);
    const LAS char* kp = kbase + lane * 16;
    const int cst = (lane & 11) == 0 ? 2048 : 2064;
    const LAS char* vp0 = kbase + (dl ? 1024 + 256 * hh + 16 * (lane & 15) : cst);
    const LAS char* vp1 = kbase + (dl ? 1536 + 256 * hh + 16 * (lane & 15) : cst);
    f32x16 nm;
    {
        const f32x16 z = {};
        const f32x16 s0 = mfma32(lds_rd(lds, lane * 16), qf, z);
        float m = s0[0];
#pragma unroll
        for (int r = 1; r < 16; ++r) m = fmaxf(m, s0[r]);
        m = fmaxf(m, xhalf(m));
#pragma unroll
        for (int r = 0; r < 16; ++r) nm[r] = -m;
    }
    f32x16 o = {};
    f32x16 sA = mfma32(*(const LAS u32x4*)kp, qf, nm), sB;
    u32x4 kf = *(const LAS u32x4*)(kp + KVT), v0 = *(const LAS u32x4*)vp0, v1 = {0u, 0u, 0u, 0u}, p0, p1 = {0u, 0u, 0u, 0u};
    SB();
#define TILE(C, N, u) do { \
    N = mfma32(kf, qf, nm); kf = *(const LAS u32x4*)(kp + ((u) + 2) * KVT); SB(); \
    C[0] = EX(C[0]); C[1] = EX(C[1]); C[2] = EX(C[2]); C[3] = EX(C[3]); SB(); p0[0] = pk2(C[0], C[1]); SB(); C[4] = EX(C[4]); SB(); p0[1] = pk2(C[2], C[3]); SB(); C[5] = EX(C[5]); SB(); \
    o = mfma32(v1, p1, o); v1 = *(const LAS u32x4*)(vp1 + (u) * KVT); SB(); \
    C[6] = EX(C[6]); C[7] = EX(C[7]); SB(); p0[2] = pk2(C[4], C[5]); SB(); C[8] = EX(C[8]); C[9] = EX(C[9]); SB(); p0[3] = pk2(C[6], C[7]); SB(); C[10] = EX(C[10]); C[11] = EX(C[11]); SB(); \
    o = mfma32(v0, p0, o); v0 = *(const LAS u32x4*)(vp0 + ((u) + 1) * KVT); SB(); \
    p1[0] = pk2(C[8], C[9]); SB(); C[12] = EX(C[12]); C[13] = EX(C[13]); SB(); p1[1] = pk2(C[10], C[11]); SB(); C[14] = EX(C[14]); C[15] = EX(C[15]); SB(); \
    p1[2] = pk2(C[12], C[13]); SB(); p1[3] = pk2(C[14], C[15]); SB(); } while (0)
#define TILE2(u) do { TILE(sA, sB, u); TILE(sB, sA, (u) + 1); PIN(p1); SB(); } while (0)
#define ADV(n) do { kp += (n) * KVT; vp0 += (n) * KVT; vp1 += (n) * KVT; } while (0)
    TILE2(0);
    DMA_B(2); DMA_B(3); DMA_B(4);
    TILE2(2);
    DMA_B(5); DMA_B(6);
    ST(3); WAITV_BAR(5); ST(4);
    TILE2(4); TILE2(6);
    WAITV_BAR(0);
    ADV(8);
    u32x8 pf0, pf1;
    {
        int ln = lane; PIN(ln);
        const int n = ln & 31, h4 = 4 * (ln >> 5);
        const int f = (b - 112) * 16 + w, fd = f - 512;
        const u16* s0 = b < 112 ? (n < 16 ? wg : wu) + (size_t)(16 * w + h4) * F_ + 16 * b + (n & 15)
                      : f < 512 ? wo + (size_t)(16 * (f >> 4) + h4) * D_ + 32 * (f & 15) + n
                                : wd + (size_t)(16 * (fd >> 4) + h4) * D_ + 32 * (fd & 15) + n;
        pf0 = gather_async<256>(s0, b < 112 ? F_ : D_, b);
        pf1 = gather_async<112>(s0 + 256 * F_, F_, b);
    }
    ST(5);
#pragma unroll 1
    for (int it = 0; it < 3; ++it) { TILE2(0); TILE2(2); ADV(4); }
    {
        asm volatile("s_waitcnt vmcnt(0)" : "+v"(pf0), "+v"(pf1) :: "memory");
        int ln = lane; PIN(ln);
        const rsrc_t rs = ws_rsrc(ws);
        const u32x4 f0 = pack8(pf0);
        if (b < 112) {
            const u32x4 f1 = pack8(pf1);
            st16(rs, WS_WGU + (b * 32 + w) * 1024 + ln * 16, f0);
            st16(rs, WS_WGU + (b * 32 + 16 + w) * 1024 + ln * 16, f1);
            const int col = ln & 31, h4 = 4 * (ln >> 5);
            u32x4 b0 = {0u, 0u, 0u, 0u}, b1 = {0u, 0u, 0u, 0u};
            if (col < 2) {
                const float* src = (const float*)(ws + WS_MOD) + (col == 0 ? 2048 : 1536) + 16 * w + h4;
                f32x4 a0 = *(const f32x4*)src, a1 = *(const f32x4*)(src + 8), a2 = *(const f32x4*)(src + 256), a3 = *(const f32x4*)(src + 264);
                if (col == 0) { a0 += 1.f; a1 += 1.f; a2 += 1.f; a3 += 1.f; }
                b0 = u32x4{pk2(a0[0], a0[1]), pk2(a0[2], a0[3]), pk2(a1[0], a1[1]), pk2(a1[2], a1[3])};
                b1 = u32x4{pk2(a2[0], a2[1]), pk2(a2[2], a2[3]), pk2(a3[0], a3[1]), pk2(a3[2], a3[3])};
            }
            const f32x16 z = {};
            const f32x16 cc = mfma32(f1, b1, mfma32(f0, b0, z));
            if (col < 2) {
                float* cx = (float*)(lds + P2_C) + (w * 2 + col) * 32 + h4;
#pragma unroll
                for (int q = 0; q < 4; ++q) *(f32x4*)(cx + 8 * q) = f32x4{cc[4 * q], cc[4 * q + 1], cc[4 * q + 2], cc[4 * q + 3]};
            }
        } else {
            const int f = (b - 112) * 16 + w, fd = f - 512;
            st16(rs, (f < 512 ? WS_WO + ((f & 15) * 32 + (f >> 4)) * 1024 : WS_WD + ((fd & 15) * 112 + (fd >> 4)) * 1024) + ln * 16, f0);
        }
        if (tid == 0) *flag = 0;
        SB();
    }
#pragma unroll 1
    for (int it = 0; it < 3; ++it) { TILE2(0); TILE2(2); ADV(4); }
    o = mfma32(v1, p1, o);
    ST(6);
    for (int attempt = 0;; ++attempt) {
        if (kh == 1) {
#pragma unroll
            for (int r = 0; r < 9; ++r) xch[(qt * 9 + r) * 64 + lane] = o[r];
        }
        BAR();
        if (kh == 0) {
#pragma unroll
            for (int r = 0; r < 9; ++r) o[r] += xch[(qt * 9 + r) * 64 + lane];
            bool bad = !(o[8] <= 1.0e12f);
#pragma unroll
            for (int r = 0; r < 8; ++r) bad |= !(fabsf(o[r]) <= 3.0e38f);
            if (__any(bad)) *flag = 1;
            const float rl = __builtin_amdgcn_rcpf(o[8]);
            u32x4 ov = {pk2(o[0] * rl, o[1] * rl), pk2(o[2] * rl, o[3] * rl), pk2(o[4] * rl, o[5] * rl), pk2(o[6] * rl, o[7] * rl)};
            st16(ws_rsrc(ws), WS_O + ((qb * 8 + qt) * 32 + head) * 1024 + lane * 16, ov);
        } else if (w == 8 && b < 112) {
            const float* cx = (const float*)(lds + P2_C) + lane;
            float s = 0.f;
#pragma unroll
            for (int i = 0; i < 16; ++i) s += cx[i * 64];
            st4(ws_rsrc(ws), WS_C12 + (b * 64 + lane) * 4, __float_as_uint(s));
        }
        if (attempt == 1) break;
        BAR();
        if (*flag == 0) break;
        ADV(-32);
        const f32x16 z = {};
        float mx = -INFINITY;
#pragma unroll 1
        for (int t = 0; t < 32; ++t) {
            const f32x16 s0 = mfma32(*(const LAS u32x4*)(kp + t * KVT), qf, z);
#pragma unroll
            for (int r = 0; r < 16; ++r) mx = fmaxf(mx, s0[r]);
        }
        mx = fmaxf(mx, xhalf(mx));
        asm volatile("s_waitcnt vmcnt(0)" ::: "memory");
        xch[w * 64 + lane] = mx;
        BAR();
        mx = fmaxf(mx, xch[(w ^ 8) * 64 + lane]);
        BAR();
        o = z;
#pragma unroll 1
        for (int t = 0; t < 32; ++t) {
            const f32x16 s0 = mfma32(*(const LAS u32x4*)(kp + t * KVT), qf, z);
#pragma unroll
            for (int j = 0; j < 4; ++j) {
                p0[j] = pk2(EX(s0[2 * j] - mx), EX(s0[2 * j + 1] - mx));
                p1[j] = pk2(EX(s0[8 + 2 * j] - mx), EX(s0[8 + 2 * j + 1] - mx));
            }
            o = mfma32(*(const LAS u32x4*)(vp0 + t * KVT), p0, o);
            o = mfma32(*(const LAS u32x4*)(vp1 + t * KVT), p1, o);
        }
    }
    ST(7); ST_FLUSH(2);
}

__device__ __forceinline__ void reduce4(f32x16 (&acc)[4], char* lds, int w, int lane) {
#pragma unroll
    for (int i = 1; i < 4; ++i)
#pragma unroll
        for (int q = 0; q < 4; ++q) {
            f32x4 v = {acc[i][4 * q], acc[i][4 * q + 1], acc[i][4 * q + 2], acc[i][4 * q + 3]};
            *(LAS f32x4*)((LAS char*)lds + (((w ^ i) * 3 + (i - 1)) * 4 + q) * 1024 + lane * 16) = v;
        }
    BAR();
#pragma unroll
    for (int i = 1; i < 4; ++i)
#pragma unroll
        for (int q = 0; q < 4; ++q) {
            const f32x4 v = *(const LAS f32x4*)((const LAS char*)lds + ((w * 3 + (i - 1)) * 4 + q) * 1024 + lane * 16);
            acc[0][4 * q] += v[0]; acc[0][4 * q + 1] += v[1]; acc[0][4 * q + 2] += v[2]; acc[0][4 * q + 3] += v[3];
        }
}

__global__ __launch_bounds__(256) void p3_wo(const u16* __restrict__ x, char* __restrict__ ws) {
    __shared__ __attribute__((aligned(16))) char lds[49152];
    const int b = blockIdx.x, tid = threadIdx.x, lane = tid & 63, hh = lane >> 5;
    const int w = __builtin_amdgcn_readfirstlane(tid >> 6);
    const int i5 = b >> 3;
    const int tt = (b & 7) * 8 + (i5 >> 3) * 2 + (w & 1), nt = (i5 & 7) * 2 + (w >> 1);
    ST_DECL; ST(0);
    const char* a0p = ws + WS_WO + (size_t)(nt * 32 + 8 * w) * 1024 + lane * 16;
    const char* a1p = ws + WS_WO + (size_t)((nt ^ 1) * 32 + 8 * w) * 1024 + lane * 16;
    const char* b0p = ws + WS_O + (size_t)(tt * 32 + 8 * w) * 1024 + lane * 16;
    const char* b1p = ws + WS_O + (size_t)((tt ^ 1) * 32 + 8 * w) * 1024 + lane * 16;
    u32x4 a0[8], a1[8], b0[8], b1[8];
#pragma unroll
    for (int k = 0; k < 8; ++k) {
        b0[k] = *(const u32x4*)(b0p + k * 1024); a0[k] = *(const u32x4*)(a0p + k * 1024);
        b1[k] = *(const u32x4*)(b1p + k * 1024); a1[k] = *(const u32x4*)(a1p + k * 1024);
    }
    const int tok = 32 * tt + (lane & 31);
    const float* mod = (const float*)(ws + WS_MOD);
    u32x2 xr[4]; f32x4 ga[4], cb[4];
#pragma unroll
    for (int q = 0; q < 4; ++q) {
        const int n = 32 * nt + 8 * q + 4 * hh;
        xr[q] = *(const u32x2*)(x + (size_t)tok * D_ + n);
        ga[q] = *(const f32x4*)(mod + 1024 + n); cb[q] = *(const f32x4*)(mod + 2048 + n);
    }
    f32x16 acc[4] = {};
    ST(1);
#pragma unroll
    for (int k = 0; k < 8; ++k) {
        acc[0] = mfma32(a0[k], b0[k], acc[0]); acc[1] = mfma32(a0[k], b1[k], acc[1]);
        acc[2] = mfma32(a1[k], b0[k], acc[2]); acc[3] = mfma32(a1[k], b1[k], acc[3]);
    }
    ST(2);
    reduce4(acc, lds, w, lane);
    ST(3);
    float x1[16]; unsigned x1p[8], yp[8];
#pragma unroll
    for (int q = 0; q < 4; ++q) {
        x1p[2 * q] = pk2(fmaf(ga[q][0], acc[0][4 * q], bflo(xr[q][0])), fmaf(ga[q][1], acc[0][4 * q + 1], bfhi(xr[q][0])));
        x1p[2 * q + 1] = pk2(fmaf(ga[q][2], acc[0][4 * q + 2], bflo(xr[q][1])), fmaf(ga[q][3], acc[0][4 * q + 3], bfhi(xr[q][1])));
        x1[4 * q] = bflo(x1p[2 * q]); x1[4 * q + 1] = bfhi(x1p[2 * q]); x1[4 * q + 2] = bflo(x1p[2 * q + 1]); x1[4 * q + 3] = bfhi(x1p[2 * q + 1]);
        yp[2 * q] = pk2(x1[4 * q] * rbf(1.f + cb[q][0]), x1[4 * q + 1] * rbf(1.f + cb[q][1]));
        yp[2 * q + 1] = pk2(x1[4 * q + 2] * rbf(1.f + cb[q][2]), x1[4 * q + 3] * rbf(1.f + cb[q][3]));
    }
#pragma unroll
    for (int s = 0; s < 2; ++s) {
        const unsigned off = (tt * 32 + 2 * nt + s) * 1024 + lane * 16;
        st16(ws_rsrc(ws), WS_Y + off, u32x4{yp[4 * s], yp[4 * s + 1], yp[4 * s + 2], yp[4 * s + 3]});
        st16(ws_rsrc(ws), WS_X1 + off, u32x4{x1p[4 * s], x1p[4 * s + 1], x1p[4 * s + 2], x1p[4 * s + 3]});
    }
    float s = 0.f;
#pragma unroll
    for (int r = 0; r < 16; ++r) s += x1[r];
    s += xhalf(s);
    const float mean = s * (1.f / 32.f);
    float q2 = 0.f;
#pragma unroll
    for (int r = 0; r < 16; ++r) { const float d = x1[r] - mean; q2 += d * d; }
    q2 += xhalf(q2);
    if (hh == 0) st8(ws_rsrc(ws), WS_STAT + ((tt * 16 + nt) * 32 + lane) * 8, u32x2{__float_as_uint(mean), __float_as_uint(q2)});
    ST(7); ST_FLUSH(3);
}

constexpr int P4_LDS = 131072 + 4096 + 1024;
__global__ __launch_bounds__(512) void p4_gu(char* __restrict__ ws) {
    extern __shared__ __attribute__((aligned(16))) char lds[];
    const int b = blockIdx.x, tid = threadIdx.x, lane = tid & 63, hh = lane >> 5;
    const int w = __builtin_amdgcn_readfirstlane(tid >> 6);
    const int i5 = b >> 3, rp = 8 * (b & 1) + (i5 >> 2), cg = 4 * ((b & 7) >> 1) + (i5 & 3);
    ST_DECL; ST(0);
    if (w == 0) {
        __builtin_amdgcn_s_setprio(3);
        const unsigned l0 = (unsigned)(uintptr_t)lds;
        const char* src = ws + WS_Y + (size_t)rp * 4 * 32768;
#pragma unroll 1
        for (int st = 0; st < 8; ++st) {
#pragma unroll
            for (int i = 0; i < 16; ++i) {
                const int ks = st * 4 + (i >> 2), t = i & 3;
                glds16(src + t * 32768 + ks * 1024, lane * 16, l0 + (ks * 4 + t) * 1024);
            }
            if (st == 1) { ST(1); WAITV_BAR(16); ST(2); }
            if (st == 3) ST(3);
            if (st == 5) ST(4);
            if (st == 7) ST(5);
            if (st > 2) WAITV_BAR(32);
        }
        WAITV_BAR(16);
        WAITV_BAR(0);
        ST(6);
#pragma unroll
        for (int p = 0; p < 2; ++p) {
            const char* sp = ws + WS_STAT + (size_t)((4 * rp + 2 * p + hh) * 16 * 32 + (lane & 31)) * 8;
            f32x2 sv[16];
#pragma unroll
            for (int i = 0; i < 16; ++i) sv[i] = *(const f32x2*)(sp + 256 * i);
            float s = 0.f;
#pragma unroll
            for (int i = 0; i < 16; ++i) s += sv[i][0];
            const float mean = s * (1.f / 16.f);
            float q = 0.f;
#pragma unroll
            for (int i = 0; i < 16; ++i) { const float d = sv[i][0] - mean; q += fmaf(32.f * d, d, sv[i][1]); }
            const float rs = __builtin_amdgcn_rsqf(q * (1.f / 512.f) + 1e-5f);
            *(LAS f32x2*)((LAS char*)lds + 131072 + 4096 + (64 * p + lane) * 8) = f32x2{rs, -rs * mean};
        }
        BAR();
        ST(7); ST_FLUSH(4);
        return;
    }
    const int nt = cg * 7 + w - 1;
    const rsrc_t wp = mk_rsrc(ws + WS_WGU + (size_t)nt * 32768, 32768);
    u32x4 wf[16];
#pragma unroll
    for (int ks = 0; ks < 16; ++ks) wf[ks] = ld16(wp, lane * 16 + (ks & 3) * 1024, (ks >> 2) * 4096);
    const float* c12 = (const float*)(ws + WS_C12) + nt * 64 + 4 * hh;
    f32x4 c1[4], c2[4];
#pragma unroll
    for (int q = 0; q < 4; ++q) { c1[q] = *(const f32x4*)(c12 + 8 * q); c2[q] = *(const f32x4*)(c12 + 32 + 8 * q); }
    SB();
    f32x16 acc[4] = {};
    const LAS char* yb = (const LAS char*)lds + lane * 16;
    ST(1);
    BAR();
    ST(2);
    u32x4 yf[4];
#pragma unroll
    for (int t = 0; t < 4; ++t) yf[t] = *(const LAS u32x4*)(yb + t * 1024);
#pragma unroll 1
    for (int it = 0; it < 2; ++it) {
#pragma unroll
        for (int u = 0; u < 16; ++u) {
            if ((u & 3) == 3 && (u < 15 || it == 0)) BARE();
            if (u == 15) yb += 65536;
#pragma unroll
            for (int t = 0; t < 4; ++t) { acc[t] = mfma32(wf[u], yf[t], acc[t]); yf[t] = *(const LAS u32x4*)(yb + (((u + 1) & 15) * 4 + t) * 1024); SB(); }
            wf[u] = ld16(wp, lane * 16 + (u & 3) * 1024, it * 16384 + 16384 + (u >> 2) * 4096);
            SB();
        }
    }
    ST(6);
    BAR();
#pragma unroll
    for (int t = 0; t < 4; ++t) {
        const f32x2 rr = *(const LAS f32x2*)((const LAS char*)lds + 131072 + 4096 + (32 * t + (lane & 31)) * 8);
        float a[8];
#pragma unroll
        for (int r = 0; r < 8; ++r) {
            const float g = fmaf(acc[t][r], rr[0], fmaf(rr[1], c1[r >> 2][r & 3], c2[r >> 2][r & 3]));
            const float u = fmaf(acc[t][8 + r], rr[0], fmaf(rr[1], c1[2 + (r >> 2)][r & 3], c2[2 + (r >> 2)][r & 3]));
            const float e = __builtin_amdgcn_exp2f(-1.4426950408889634f * g);
            a[r] = g * u * __builtin_amdgcn_rcpf(1.f + e);
        }
        st16(ws_rsrc(ws), WS_ACT + ((4 * rp + t) * 112 + nt) * 1024 + lane * 16,
             u32x4{pk2(a[0], a[1]), pk2(a[2], a[3]), pk2(a[4], a[5]), pk2(a[6], a[7])});
    }
    ST(7); ST_FLUSH(4);
}

__global__ __launch_bounds__(256) void p5_down(char* __restrict__ ws, u16* __restrict__ out) {
    __shared__ __attribute__((aligned(16))) char lds[49152];
    const int b = blockIdx.x, tid = threadIdx.x, lane = tid & 63, hh = lane >> 5;
    const int w = __builtin_amdgcn_readfirstlane(tid >> 6);
    const int i5 = b >> 3;
    const int tt = (b & 7) * 8 + (i5 >> 3) * 2 + (w & 1), nt = (i5 & 7) * 2 + (w >> 1);
    ST_DECL; ST(0);
    const char* a0p = ws + WS_WD + (size_t)(nt * 112 + 28 * w) * 1024 + lane * 16;
    const char* a1p = ws + WS_WD + (size_t)((nt ^ 1) * 112 + 28 * w) * 1024 + lane * 16;
    const char* b0p = ws + WS_ACT + (size_t)(tt * 112 + 28 * w) * 1024 + lane * 16;
    const char* b1p = ws + WS_ACT + (size_t)((tt ^ 1) * 112 + 28 * w) * 1024 + lane * 16;
    constexpr int WIN = 12;
    u32x4 a0[28], a1[28], b0[28], b1[28];
#pragma unroll
    for (int k = 0; k < WIN; ++k) {
        b0[k] = *(const u32x4*)(b0p + k * 1024); a0[k] = *(const u32x4*)(a0p + k * 1024);
        b1[k] = *(const u32x4*)(b1p + k * 1024); a1[k] = *(const u32x4*)(a1p + k * 1024);
    }
    __builtin_amdgcn_sched_barrier(0);
    ST(1);
    f32x16 acc[4] = {};
#pragma unroll
    for (int k = 0; k < 28; ++k) {
        acc[0] = mfma32(a0[k], b0[k], acc[0]); acc[1] = mfma32(a0[k], b1[k], acc[1]);
        acc[2] = mfma32(a1[k], b0[k], acc[2]); acc[3] = mfma32(a1[k], b1[k], acc[3]);
        if (k + WIN < 28) {
            const int n = k + WIN;
            b0[n] = *(const u32x4*)(b0p + n * 1024); a0[n] = *(const u32x4*)(a0p + n * 1024);
            b1[n] = *(const u32x4*)(b1p + n * 1024); a1[n] = *(const u32x4*)(a1p + n * 1024);
        }
        __builtin_amdgcn_sched_barrier(0);
    }
    ST(2);
    const float* mod = (const float*)(ws + WS_MOD);
    u32x4 xr[2]; f32x4 gb[4];
#pragma unroll
    for (int s = 0; s < 2; ++s) xr[s] = *(const u32x4*)(ws + WS_X1 + (size_t)(tt * 32 + 2 * nt + s) * 1024 + lane * 16);
#pragma unroll
    for (int q = 0; q < 4; ++q) gb[q] = *(const f32x4*)(mod + 2560 + 32 * nt + 8 * q + 4 * hh);
    reduce4(acc, lds, w, lane);
    ST(3);
    u16* orow = out + (size_t)(32 * tt + (lane & 31)) * D_ + 32 * nt + 4 * hh;
#pragma unroll
    for (int q = 0; q < 4; ++q) {
        const unsigned x0 = xr[q >> 1][2 * (q & 1)], x1 = xr[q >> 1][2 * (q & 1) + 1];
        u32x2 o = {pk2(fmaf(gb[q][0], acc[0][4 * q], bflo(x0)), fmaf(gb[q][1], acc[0][4 * q + 1], bfhi(x0))),
                   pk2(fmaf(gb[q][2], acc[0][4 * q + 2], bflo(x1)), fmaf(gb[q][3], acc[0][4 * q + 3], bfhi(x1)))};
        *(u32x2*)(orow + 8 * q) = o;
    }
    ST(7); ST_FLUSH(5);
}

extern "C" void kernel_launch(void* const* d_in, const int* in_sizes, int n_in, void* d_out, int out_size, void* d_ws, size_t ws_size,
                              hipStream_t stream) {
    const u16* x = (const u16*)d_in[0];
    const u16* wqkv = (const u16*)d_in[1];
    const u16* wo = (const u16*)d_in[2];
    const u16* wg = (const u16*)d_in[4];
    const u16* wu = (const u16*)d_in[5];
    const u16* wd = (const u16*)d_in[6];
    const u16* c = (const u16*)d_in[7];
    const u16* wmod = (const u16*)d_in[8];
    char* ws = (char*)d_ws;
    (void)hipFuncSetAttribute((const void*)p1_qkv, hipFuncAttributeMaxDynamicSharedMemorySize, P1_LDS);
    (void)hipFuncSetAttribute((const void*)p2_attn, hipFuncAttributeMaxDynamicSharedMemorySize, P2_LDS);
    (void)hipFuncSetAttribute((const void*)p4_gu, hipFuncAttributeMaxDynamicSharedMemorySize, P4_LDS);
    k0_prep<<<256, 512, 0, stream>>>(x, wqkv, c, wmod, ws);
    p1_qkv<<<256, 512, P1_LDS, stream>>>(ws);
    p2_attn<<<256, 1024, P2_LDS, stream>>>(wo, wg, wu, wd, ws);
    p3_wo<<<256, 256, 0, stream>>>(x, ws);
    p4_gu<<<256, 512, P4_LDS, stream>>>(ws);
    p5_down<<<256, 256, 0, stream>>>(ws, (u16*)d_out);
}
```

```cpp
#include <hip/hip_runtime.h>
#include <hip/hip_bf16.h>
#include <stdint.h>

typedef __attribute__((ext_vector_type(8))) __bf16 bf16x8;
typedef __attribute__((ext_vector_type(2))) __bf16 bf16x2;
typedef __attribute__((ext_vector_type(2))) float f32x2;
typedef __attribute__((ext_vector_type(4))) float f32x4;
typedef __attribute__((ext_vector_type(16))) float f32x16;
typedef __attribute__((ext_vector_type(4))) unsigned u32x4;
typedef __attribute__((ext_vector_type(2))) unsigned u32x2;
typedef unsigned short u16;
#define LAS __attribute__((address_space(3)))
#if defined(__HIP_DEVICE_COMPILE__)
#define NOPK __attribute__((target("no-packed-fp32-ops")))
#else
#define NOPK
#endif
#define DEV NOPK __device__ __forceinline__
#define F2U(x) __builtin_bit_cast(unsigned, (float)(x))
#define U2F(x) __builtin_bit_cast(float, (unsigned)(x))

constexpr int D_ = 512, F_ = 1792;
constexpr int KVT = 2080;
constexpr int GUT = 33 * 1024;
constexpr unsigned WS_MODP = 0;
constexpr unsigned WS_CNT = 65536;
constexpr unsigned WS_STAT = 196608;
constexpr unsigned WS_XH = WS_STAT + 131072;
constexpr unsigned WS_WQKV = WS_XH + 2097152;
constexpr unsigned WS_KV = WS_WQKV + 1572864;
constexpr unsigned WS_O = WS_KV + 4194304;
constexpr unsigned WS_WO = WS_O + 2097152;
constexpr unsigned WS_WGU = WS_WO + 524288;
constexpr unsigned WS_WD = WS_WGU + 112 * GUT;
constexpr unsigned WS_Y = WS_WD + 1835008;
constexpr unsigned WS_ACT = WS_Y + 2097152;
constexpr unsigned WS_END = WS_ACT + 64 * 112 * 1024;

DEV float bflo(unsigned u) { return U2F(u << 16); }
DEV float bfhi(unsigned u) { return U2F(u & 0xffff0000u); }
DEV unsigned pk2(float a, float b) {
    f32x2 f = {a, b};
    return __builtin_bit_cast(unsigned, __builtin_convertvector(f, bf16x2));
}
DEV float rbf(float a) { return (float)(__bf16)a; }
DEV f32x16 mfma32(u32x4 a, u32x4 b, f32x16 c) {
    return __builtin_amdgcn_mfma_f32_32x32x16_bf16(__builtin_bit_cast(bf16x8, a), __builtin_bit_cast(bf16x8, b), c, 0, 0, 0);
}
DEV f32x4 mfma16(u32x4 a, u32x4 b, f32x4 c) {
    return __builtin_amdgcn_mfma_f32_16x16x32_bf16(__builtin_bit_cast(bf16x8, a), __builtin_bit_cast(bf16x8, b), c, 0, 0, 0);
}
DEV float hsum(float v) {
    auto r = __builtin_amdgcn_permlane32_swap(F2U(v), F2U(v), false, false);
    return U2F(r[0]) + U2F(r[1]);
}
DEV float hmax(float v) {
    auto r = __builtin_amdgcn_permlane32_swap(F2U(v), F2U(v), false, false);
    return __builtin_fmaxf(U2F(r[0]), U2F(r[1]));
}
DEV void glds16(const char* g, unsigned voff, unsigned lds_base) {
    unsigned sv;
    asm volatile("s_mov_b32 %0, m0\n\ts_mov_b32 m0, %3\n\ts_nop 0\n\tglobal_load_lds_dwordx4 %1, %2\n\ts_mov_b32 m0, %0"
                 : "=&s"(sv) : "v"(voff), "s"(g), "s"(lds_base) : "memory");
}
DEV void glds16_dev(const char* g, unsigned voff, unsigned lds_base) {
    unsigned sv;
    asm volatile("s_mov_b32 %0, m0\n\ts_mov_b32 m0, %3\n\ts_nop 0\n\tglobal_load_lds_dwordx4 %1, %2 sc1\n\ts_mov_b32 m0, %0"
                 : "=&s"(sv) : "v"(voff), "s"(g), "s"(lds_base) : "memory");
}
typedef __amdgpu_buffer_rsrc_t rsrc_t;
DEV rsrc_t mk_rsrc(const void* p, unsigned bytes) { return __builtin_amdgcn_make_buffer_rsrc((void*)p, 0, bytes, 0x00020000); }
DEV rsrc_t ws_rsrc(char* ws) { return mk_rsrc(ws, WS_END); }
DEV void st16(rsrc_t r, unsigned off, u32x4 v) { __builtin_amdgcn_raw_buffer_store_b128(v, r, off, 0, 16); }
DEV void st16s(rsrc_t r, unsigned voff, unsigned soff, u32x4 v) { st16(r, voff + soff, v); }
DEV void st8(rsrc_t r, unsigned off, u32x2 v) { __builtin_amdgcn_raw_buffer_store_b64(v, r, off, 0, 16); }
DEV void st4(rsrc_t r, unsigned off, unsigned v) { __builtin_amdgcn_raw_buffer_store_b32(v, r, off, 0, 16); }
DEV u32x4 ld16(rsrc_t r, unsigned voff, unsigned soff) { return __builtin_amdgcn_raw_buffer_load_b128(r, voff, soff, 0); }
#define SB() __builtin_amdgcn_sched_barrier(0)
#define PIN(x) asm volatile("" : "+v"(x))
#define BAR() asm volatile("s_waitcnt lgkmcnt(0)\n\ts_barrier" ::: "memory")
#define BARE() asm volatile("s_barrier" ::: "memory")
#define WAITV_BAR(N) asm volatile("s_waitcnt vmcnt(" #N ") lgkmcnt(0)\n\ts_barrier" ::: "memory")
#define LDS16(p) (*(const LAS u32x4*)(p))
#define EX(x) __builtin_amdgcn_exp2f(x)
#define F4(C, o) f32x4{C[o], C[o + 1], C[o + 2], C[o + 3]}

typedef short s16x4 __attribute__((ext_vector_type(4)));
DEV u32x4 tr_frag(const LAS char* p) {
    const u32x2 a = __builtin_bit_cast(u32x2, __builtin_amdgcn_ds_read_tr16_b64_v4i16((LAS s16x4*)p));
    const u32x2 c = __builtin_bit_cast(u32x2, __builtin_amdgcn_ds_read_tr16_b64_v4i16((LAS s16x4*)(p + 512)));
    return u32x4{a[0], a[1], c[0], c[1]};
}

#define DPP_ADD(v, ctrl) ((v) + U2F(__builtin_amdgcn_update_dpp(0u, F2U(v), ctrl, 0xF, 0xF, true)))
DEV float sum16_32(float v) {
    auto r = __builtin_amdgcn_permlane16_swap(F2U(v), F2U(v), false, false);
    return hsum(U2F(r[0]) + U2F(r[1]));
}
DEV float wave_sum(float v) {
    v = DPP_ADD(v, 0xB1); v = DPP_ADD(v, 0x4E); v = DPP_ADD(v, 0x141); v = DPP_ADD(v, 0x140);
    return sum16_32(v);
}
NOPK __global__ __launch_bounds__(1024) void k0_prep(const u16* __restrict__ x, const u16* __restrict__ wqkv, const u16* __restrict__ c,
                                                     const u16* __restrict__ wmod, char* __restrict__ ws) {
    __shared__ __attribute__((aligned(16))) char lds[9216 + 1024 + 6144 + 1024];
    const int b = blockIdx.x, tid = threadIdx.x, lane = tid & 63;
    const int w = __builtin_amdgcn_readfirstlane(tid >> 6);
    const bool gv = w < 4, ln = !gv && w < 12;
    const int f0 = 3 * b + w - 12, np = f0 >> 5, ks = f0 & 31;
    const int row0 = 128 * (b & 3) + 32 * w;
    const rsrc_t r0 = mk_rsrc(gv ? wmod : ln ? x : wqkv, gv ? (b < 192 ? 512 * 3072 * 2 : 0) : ln ? 2048 * D_ * 2 : w < 15 ? 512 * 1536 * 2 : 0);
    const unsigned sh = gv ? 3 : ln ? 6 : 2, pitch = gv ? 6144 : 3072;
    const unsigned voff = (lane >> sh) * pitch + (lane & ((1u << sh) - 1)) * 16;
    const unsigned so = gv ? row0 * 6144 + (b >> 2) * 128 : ln ? (8 * b + w - 4) * 1024 : ks * 49152 + np * 128;
    const unsigned OOB = 0x40000000u;
    u32x4 v[4]; unsigned cr[4];
    v[0] = ld16(r0, voff, so);
    v[1] = ld16(r0, voff, gv ? so + 49152 : ln ? OOB : so + 64);
    v[2] = ld16(r0, voff, gv ? so + 98304 : OOB);
    v[3] = ld16(r0, voff, gv ? so + 147456 : OOB);
#pragma unroll
    for (int i = 0; i < 4; ++i) cr[i] = __builtin_amdgcn_raw_buffer_load_b16(mk_rsrc(c, gv ? 1024 : 0), (lane >> 3) * 2 + 16 * i, row0 * 2, 0);
    SB();
    const rsrc_t rs = ws_rsrc(ws);
    if (gv) {
        float acc[8];
#pragma unroll
        for (int j = 0; j < 8; ++j) acc[j] = 0.f;
#pragma unroll
        for (int i = 0; i < 4; ++i) {
            const float sc = bflo(cr[i]), s = rbf(sc * __builtin_amdgcn_rcpf(1.f + EX(-1.4426950408889634f * sc)));
#pragma unroll
            for (int j = 0; j < 4; ++j) { acc[2 * j] = __builtin_fmaf(s, bflo(v[i][j]), acc[2 * j]); acc[2 * j + 1] = __builtin_fmaf(s, bfhi(v[i][j]), acc[2 * j + 1]); }
        }
#pragma unroll
        for (int j = 0; j < 8; ++j) acc[j] = sum16_32(DPP_ADD(acc[j], 0x128));
        LAS float* red = (LAS float*)((LAS char*)lds + 9216);
        if (lane < 8) { *(LAS f32x4*)(red + 64 * w + 8 * lane) = F4(acc, 0); *(LAS f32x4*)(red + 64 * w + 8 * lane + 4) = F4(acc, 4); }
        BAR();
        if (w == 0 && b < 192) st4(rs, WS_MODP + ((b & 3) * 3072 + 64 * (b >> 2) + lane) * 4, F2U((red[lane] + red[64 + lane]) + (red[128 + lane] + red[192 + lane])));
    } else if (ln) {
        float f[8];
#pragma unroll
        for (int j = 0; j < 4; ++j) { f[2 * j] = bflo(v[0][j]); f[2 * j + 1] = bfhi(v[0][j]); }
        const float mean = wave_sum(((f[0] + f[1]) + (f[2] + f[3])) + ((f[4] + f[5]) + (f[6] + f[7]))) * (1.f / 512.f);
        float q = 0.f;
#pragma unroll
        for (int j = 0; j < 8; ++j) { f[j] -= mean; q = __builtin_fmaf(f[j], f[j], q); }
        const float rr = __builtin_amdgcn_rsqf(wave_sum(q) * (1.f / 512.f) + 1e-5f);
        LAS char* d = (LAS char*)lds + (lane >> 1) * 288 + (w - 4) * 16 + (lane & 1) * 8;
        *(LAS u32x2*)d = u32x2{pk2(f[0] * rr, f[1] * rr), pk2(f[2] * rr, f[3] * rr)};
        *(LAS u32x2*)(d + 144) = u32x2{pk2(f[4] * rr, f[5] * rr), pk2(f[6] * rr, f[7] * rr)};
        BAR();
        const int pc = 8 * (w - 4) + (lane >> 3);
        st16(rs, WS_XH + (b >> 2) * 32768 + pc * 512 + (b & 3) * 128 + (lane & 7) * 16, LDS16((const LAS char*)lds + pc * 144 + (lane & 7) * 16));
    } else if (w < 15) {
        LAS char* sc = (LAS char*)lds + 10240 + (w - 12) * 2048;
        *(LAS u32x4*)(sc + lane * 16) = v[0]; *(LAS u32x4*)(sc + 1024 + lane * 16) = v[1];
        BAR();
        const LAS char* tr = sc + (4 * (lane >> 5) + ((lane & 15) >> 2)) * 64 + (lane & 16) * 2 + (lane & 3) * 8;
        const int kind = np >> 3;
        const unsigned off = WS_WQKV + (4 * (np & 7) + ((lane >> 4) & 1)) * 49152 + ks * 1536 + (lane & 15) * 16 +
                             (kind == 2 ? 1024 + (lane >> 5) * 256 : kind * 256 + (lane >> 5) * 512);
#pragma unroll
        for (int i = 0; i < 2; ++i) st16(rs, off + i * 98304, tr_frag(tr + i * 1024));
    } else {
        if (b < 240) {
            const char* pc;
            asm volatile("s_getpc_b64 vcc\n\ts_add_u32 vcc_lo, vcc_lo, m_main@rel32@lo+4\n\ts_addc_u32 vcc_hi, vcc_hi, m_main@rel32@hi+12\n\ts_mov_b64 %0, vcc"
                         : "=s"(pc) :: "scc", "vcc");
            glds16(pc + (b >> 3) * 1024, lane * 16, (unsigned)(uintptr_t)lds + 16384);
        }
        if (lane < 2) st4(rs, WS_CNT + (2 * b + lane) * 256, 0u);
        BARE();
    }
}

constexpr int P1_W = 65536, P1_T = P1_W + 49152, P1_C = P1_T + 4096;
constexpr int M_R = 135168, M_X1 = M_R + 8192, M_S = M_X1 + 8192, M_C = M_S + 2048, M_TAB = M_C + 1024, M_F = M_TAB + 768, M_T = M_TAB + 1024, M_LDS = M_T + 8192;
#define M_WD(j) ((j) < 8 ? (j) * 8192 : (j) == 8 ? M_R : M_T)
constexpr int FL_T = 64, CN_Y = 80, CN_A = 384, CN_T = 192, CN_O = 0, CN_S = 233, FL_S = 256, CN_C = 320;
typedef __attribute__((address_space(1))) unsigned gu32;
DEV void arrive(char* ws, unsigned cn, unsigned n, unsigned fl, int nfl, int lane) {
    unsigned old = 0;
    if (lane == 0) old = __hip_atomic_fetch_add((gu32*)(ws + WS_CNT + cn * 256), 1u, __ATOMIC_RELAXED, __HIP_MEMORY_SCOPE_AGENT);
    if (__builtin_amdgcn_readfirstlane(old) == n - 1 && lane < nfl)
        __hip_atomic_store((gu32*)(ws + WS_CNT + (fl + lane) * 256), 1u, __ATOMIC_RELAXED, __HIP_MEMORY_SCOPE_AGENT);
}
template <int SLEEP> DEV void fl_wait(char* ws, unsigned fl) {
#pragma unroll 1
    for (int spin = 0; spin < (1 << 17); ++spin) {
        const unsigned v = __hip_atomic_load((gu32*)(ws + WS_CNT + fl * 256), __ATOMIC_RELAXED, __HIP_MEMORY_SCOPE_AGENT);
        if (__builtin_amdgcn_readfirstlane(v) != 0) break;
        __builtin_amdgcn_s_sleep(SLEEP);
    }
    asm volatile("" ::: "memory");
}
template <int R = 4> DEV void handoff(char* ws, unsigned cn, unsigned cw, unsigned n, int h, int lane) {
    if (h == 0 && lane < R) __hip_atomic_fetch_add((gu32*)(ws + WS_CNT + (cn + lane) * 256), 1u, __ATOMIC_RELAXED, __HIP_MEMORY_SCOPE_AGENT);
    const char* a = ws + WS_CNT + (cw + h) * 256;
#pragma unroll 1
    for (int spin = 0; spin < (1 << 17); ++spin) {
        unsigned v = 0;
        if (lane == 0) asm volatile("global_atomic_or %0, %1, %0, %2 sc0\n\ts_waitcnt vmcnt(0)" : "+v"(v) : "v"(0u), "s"(a) : "memory");
        if (__builtin_amdgcn_readfirstlane(v) >= n) break;
    }
    asm volatile("" ::: "memory");
}
DEV void wait_go(volatile LAS int* go, int epoch) {
    while (__builtin_amdgcn_readfirstlane(*go) < epoch) __builtin_amdgcn_s_sleep(1);
    asm volatile("" ::: "memory");
}
DEV u32x4 ld16d(rsrc_t r, unsigned voff, unsigned soff) { return __builtin_amdgcn_raw_buffer_load_b128(r, voff, soff, 16); }
template <int NST> DEV void fetch64(const char* src, unsigned l0, int h, unsigned l16) {
#pragma unroll 1
    for (int st = 0; st < NST; ++st) {
#pragma unroll
        for (int i = 0; i < 4; ++i) glds16_dev(src + (st * 8 + 2 * i) * 1024, l16, l0 + 65536 + (st & 3) * 16384 + i * 4096 + h * 1024);
        if (st >= 2) WAITV_BAR(8);
    }
    WAITV_BAR(4);
    WAITV_BAR(0);
}
DEV void m_p5(char* lds, char* ws, u16* out, u32x4 (&wd)[22], int w, int tp, int np, unsigned l16) {
    const int kh = w >> 2, ca = (w >> 1) & 1, bt = w & 1;
    WAITV_BAR(0);
    unsigned l5 = l16; PIN(l5);
    const unsigned lx = l5 ^ (kh << 8);
#pragma unroll
    for (int j = 0; j < 4; ++j) wd[j] = LDS16((const LAS char*)lds + M_WD(j) + w * 1024 + lx);
    f32x16 c0 = {}, c1 = {};
    const LAS char* a0 = (const LAS char*)lds + 65536 + (2 * kh + bt) * 2048 + bt * 1024 + l5;
    const LAS char* a1 = (const LAS char*)lds + 65536 + (2 * kh + bt) * 2048 + (bt ^ 1) * 1024 + l5;
    const LAS char* as = (const LAS char*)lds + 65536 + kh * 2048 + bt * 1024 + l5;
    const LAS char* ws_ = (const LAS char*)lds + (2 * ca + kh) * 1024 + lx;
    LAS char* xb = (LAS char*)lds + l5;
    u32x4 bb[2][4], wa[2][4];
    f32x4 u[4], gb0, gb1; u32x4 x1p; unsigned oo;
#pragma unroll
    for (int st = 0; st <= 14; ++st) {
        if (st < 14) {
            BAR();
            const int o = (st & 3) * 16384;
            if (st < 11) { bb[st & 1][0] = LDS16(a0 + o); bb[st & 1][1] = LDS16(a1 + o); bb[st & 1][2] = LDS16(a0 + o + 8192); bb[st & 1][3] = LDS16(a1 + o + 8192); }
            else {
#pragma unroll
                for (int i = 0; i < 4; ++i) bb[st & 1][i] = LDS16(as + o + i * 4096);
            }
            if (st == 12) {
#pragma unroll
                for (int i = 0; i < 4; ++i) u[i] = *(const LAS f32x4*)(xb + w * 4096 + i * 1024);
            }
            SB();
        }
#pragma unroll
        for (int e = 0; e < 2; ++e) {
            const int sm = st - 1 + e, c = sm & 1;
            if (e == 0 ? (st > 0 && st != 13) : st == 12) {
                if (sm < 11) {
                    c1 = mfma32(wd[2 * sm], bb[c][1], c1); c0 = mfma32(wd[2 * sm], bb[c][0], c0);
                    c1 = mfma32(wd[2 * sm + 1], bb[c][3], c1); c0 = mfma32(wd[2 * sm + 1], bb[c][2], c0);
                } else {
#pragma unroll
                    for (int i = 0; i < 4; ++i) c0 = mfma32(wa[c][i], bb[c][i], c0);
                }
                if (sm >= 9 && sm < 12) {
#pragma unroll
                    for (int i = 0; i < 4; ++i) wa[c][i] = LDS16(ws_ + M_WD(2 * sm - 14 + (i >> 1)) + (i & 1) * 4096);
                }
                SB();
                if (sm == 10) {
                    *(LAS f32x4*)(xb + (w ^ 1) * 4096) = F4(c1, 0); *(LAS f32x4*)(xb + (w ^ 1) * 4096 + 1024) = F4(c1, 4);
                    *(LAS f32x4*)(xb + (w ^ 5) * 4096 + 2048) = F4(c1, 8); *(LAS f32x4*)(xb + (w ^ 5) * 4096 + 3072) = F4(c1, 12);
                    SB();
                }
                if (sm == 12) {
                    const LAS char* tb = (const LAS char*)lds + M_TAB + 512 + (32 * ca + 16 * kh) * 4 + ((l5 >> 5) & 16);
                    gb0 = *(const LAS f32x4*)tb; gb1 = *(const LAS f32x4*)(tb + 32);
                    x1p = LDS16((const LAS char*)lds + M_X1 + w * 1024 + l5);
                    oo = (32 * (2 * tp + bt) * D_ + 64 * np + 32 * ca + 16 * kh) * 2 + ((l5 & 496) << 6) + ((l5 >> 5) & 16); PIN(oo);
                    u[0] += u[2]; u[1] += u[3]; PIN(u[0]); PIN(u[1]);
                    SB();
                }
            }
        }
    }
    *(LAS f32x4*)(xb + (w ^ 4) * 2048) = F4(c0, 8); *(LAS f32x4*)(xb + (w ^ 4) * 2048 + 1024) = F4(c0, 12);
    BAR();
    const f32x4 v0 = *(const LAS f32x4*)(xb + w * 2048), v1 = *(const LAS f32x4*)(xb + w * 2048 + 1024);
    float a[8];
#pragma unroll
    for (int r = 0; r < 4; ++r) { a[r] = (c0[r] + u[0][r]) + v0[r]; a[4 + r] = (c0[4 + r] + u[1][r]) + v1[r]; }
    const unsigned o0 = pk2(__builtin_fmaf(gb0[0], a[0], bflo(x1p[0])), __builtin_fmaf(gb0[1], a[1], bfhi(x1p[0])));
    const unsigned o1 = pk2(__builtin_fmaf(gb0[2], a[2], bflo(x1p[1])), __builtin_fmaf(gb0[3], a[3], bfhi(x1p[1])));
    const unsigned o2 = pk2(__builtin_fmaf(gb1[0], a[4], bflo(x1p[2])), __builtin_fmaf(gb1[1], a[5], bfhi(x1p[2])));
    const unsigned o3 = pk2(__builtin_fmaf(gb1[2], a[6], bflo(x1p[3])), __builtin_fmaf(gb1[3], a[7], bfhi(x1p[3])));
    auto r0 = __builtin_amdgcn_permlane32_swap(o0, o2, false, false);
    auto r1 = __builtin_amdgcn_permlane32_swap(o1, o3, false, false);
    __builtin_amdgcn_raw_buffer_store_b128(u32x4{r0[0], r1[0], r0[1], r1[1]}, mk_rsrc(out, 2048 * D_ * 2), oo, 0, 0);
}

DEV unsigned gs_off(int i, unsigned l16) {
    return i == 0 ? (l16 >> 3) & 64 : i == 1 ? (l16 < 512 ? (l16 >> 2) & 64 : 128) : (l16 < 512 ? 192 : 128 + ((l16 >> 2) & 64));
}
struct GS { f32x4 g0, g1, s0, s1; };
DEV GS gs_read(const LAS char* tb) { return GS{*(const LAS f32x4*)tb, *(const LAS f32x4*)(tb + 16), *(const LAS f32x4*)(tb + 32), *(const LAS f32x4*)(tb + 48)}; }
DEV float scale_piece(LAS char* dst, u32x4 wr, const GS& t, float c2) {
    float e[8];
#pragma unroll
    for (int j = 0; j < 4; ++j) { e[2 * j] = bflo(wr[j]); e[2 * j + 1] = bfhi(wr[j]); }
    *(LAS u32x4*)dst = u32x4{pk2(e[0] * t.g0[0], e[1] * t.g0[1]), pk2(e[2] * t.g0[2], e[3] * t.g0[3]), pk2(e[4] * t.g1[0], e[5] * t.g1[1]), pk2(e[6] * t.g1[2], e[7] * t.g1[3])};
    float d = e[4] * t.s1[0];
#pragma unroll
    for (int j = 0; j < 4; ++j) { c2 = __builtin_fmaf(e[j], t.s0[j], c2); if (j) d = __builtin_fmaf(e[4 + j], t.s1[j], d); }
    return c2 + d;
}
DEV void scale_first(char* lds, u32x4 wr, int w, unsigned l16, unsigned to) {
    *(LAS float*)((LAS char*)lds + P1_C + w * 256 + (l16 >> 2)) = scale_piece((LAS char*)lds + P1_W + w * 1024 + l16, wr, gs_read((const LAS char*)lds + to), 0.f);
}

extern "C" NOPK __global__ __launch_bounds__(768, 3) void m_main(char* __restrict__ ws, const u16* __restrict__ x, const u16* __restrict__ wo, const u16* __restrict__ wg,
                                                   const u16* __restrict__ wu, const u16* __restrict__ wdn, u16* __restrict__ out) {
    extern __shared__ __attribute__((aligned(16))) char lds[];
    const int b = blockIdx.x, tid = threadIdx.x, lane = tid & 63, hh = lane >> 5;
    const int w = __builtin_amdgcn_readfirstlane(tid >> 6);
    const int g = b & 7, i5 = b >> 3;
    const int head = 8 * (g >> 1) + (i5 & 7), qb = 4 * (b & 1) + (i5 >> 3);
    const int tp = 4 * g + (i5 >> 3), np = i5 & 7;
    const int rp = 4 * (g >> 1) + (i5 >> 3), cg = 8 * (g & 1) + (i5 & 7);
    volatile LAS int* go = (volatile LAS int*)((LAS char*)lds + M_F);
    const unsigned l16 = lane * 16;
    const rsrc_t rs = ws_rsrc(ws);
    const float* mod = (const float*)(ws + WS_MODP);
    const int w3 = w / 3;
    if (w >= 8) {
        const int h = w - 8;
        const unsigned l0 = (unsigned)(uintptr_t)lds;
        __builtin_amdgcn_s_setprio(3);
        {
            const unsigned w0 = WS_WQKV + head * 49152;
            const u32x4 wr = ld16(rs, l16, w0 + w * 1024);
            BARE();
            u32x4 xf[9];
#pragma unroll
            for (int u = 0; u < 9; ++u) xf[u] = ld16(rs, l16, w0 + (4 + 3 * (u / 3) + h) * 3072 + u * 1024);
            if (h == 0 && lane < 8) go[lane] = 0;
            unsigned to = P1_T + w3 * 256 + gs_off(w - 3 * w3, l16), t3[3];
#pragma unroll
            for (int i = 0; i < 3; ++i) { t3[i] = P1_T + (4 + h) * 256 + gs_off(i, l16); PIN(t3[i]); }
            PIN(to);
            BAR();
            scale_first(lds, wr, w, l16, to);
            BAR();
            float c2[3] = {0.f, 0.f, 0.f};
#pragma unroll
            for (int j = 0; j < 3; ++j) {
                GS t[3];
#pragma unroll
                for (int i = 0; i < 3; ++i) t[i] = gs_read((const LAS char*)lds + t3[i] + j * 1024);
                SB();
#pragma unroll
                for (int i = 0; i < 3; ++i) c2[i] = scale_piece((LAS char*)lds + P1_W + (4 + h) * 3072 + l16 + j * 12288 + i * 1024, xf[3 * j + i], t[i], c2[i]);
                if (j == 1) BAR();
            }
#pragma unroll
            for (int i = 0; i < 3; ++i) *(LAS float*)((LAS char*)lds + P1_C + (12 + 3 * h + i) * 256 + lane * 4) = c2[i];
            BAR();
        }
        if (h < 3) {
            const int pt = lane >> 4;
            const LAS float* cp_ = (const LAS float*)((const LAS char*)lds + P1_C) + (lane & 15) + (h < 2 ? 16 * h + 32 * pt + 32 * (pt >> 1) : 64 + 16 * pt + 64 * (pt >> 1));
            float e[8];
#pragma unroll
            for (int i = 0; i < 8; ++i) e[i] = cp_[192 * i];
            SB();
            *(LAS float*)((LAS char*)lds + M_C + h * 64 + (lane & 15) * 4) = sum16_32(((e[0] + e[1]) + (e[2] + e[3])) + ((e[4] + e[5]) + (e[6] + e[7])));
            asm volatile("s_waitcnt lgkmcnt(0)" ::: "memory");
            if (lane == 0) __hip_atomic_fetch_add((LAS int*)(go + 6), 1, __ATOMIC_RELAXED, __HIP_MEMORY_SCOPE_WORKGROUP);
        }
        if (h == 1 && lane < 31) *(LAS u32x4*)((LAS char*)lds + lane * KVT + 2048) = u32x4{0x3F803F80u, 0x3F803F80u, 0x3F803F80u, 0x3F803F80u};
        if (h == 2 && lane < 31) *(LAS u32x4*)((LAS char*)lds + lane * KVT + 2064) = u32x4{0u, 0u, 0u, 0u};
        BAR();
        if (h == 1 && lane >= 31) *(LAS u32x4*)((LAS char*)lds + lane * KVT + 2048) = u32x4{0x3F803F80u, 0x3F803F80u, 0x3F803F80u, 0x3F803F80u};
        if (h == 2 && lane >= 31) *(LAS u32x4*)((LAS char*)lds + lane * KVT + 2064) = u32x4{0u, 0u, 0u, 0u};
        wait_go(go + 4, 8);
        handoff(ws, CN_A + 4 * head, CN_A + 4 * head, 8, h, lane);
        {
            const char* kv = ws + WS_KV + (size_t)head * 131072 + h * 1024;
            const unsigned d0 = l0 + (8 + (h >> 1)) * KVT + (h & 1) * 1024;
#pragma unroll
            for (int i = 0; i < 28; ++i) {
                glds16_dev(kv + ((4 * qb + 4 + i) & 31) * 4096, l16, d0 + i * 2 * KVT);
                if (i == 10) WAITV_BAR(6);
            }
            WAITV_BAR(0);
        }
        u32x4 rw[8]; float mv[8], tv[4];
        {
            const int ks = b < 144 ? b - 112 : b - 144;
            const unsigned ld2 = b < 112 ? 2 * F_ : 2 * D_, voff = (lane >> 2) * ld2 + (lane & 3) * 16;
            const unsigned so = b < 112 ? (16 * (b & 1) + h) * 32 * F_ + (b >> 1) * 64 : ks * 32 * D_ + h * 256, str = b < 112 ? 128 * F_ : 64;
            const rsrc_t ra = mk_rsrc(b < 112 ? wg : b < 144 ? wo : wdn, b >= 112 && b < 144 ? D_ * D_ * 2 : D_ * F_ * 2), rb = mk_rsrc(wu, b < 112 ? D_ * F_ * 2 : 0);
#pragma unroll
            for (int i = 0; i < 4; ++i) { rw[i] = ld16(ra, voff, so + i * str); rw[4 + i] = ld16(rb, voff, so + i * str); }
            const float* mp = mod + 16 * (16 * (b & 1) + 4 * (lane >> 4) + h) + (lane & 15);
            const int k = (h == 0 ? 1024 : h == 1 ? 2048 : 2560) + 64 * np + lane;
#pragma unroll
            for (int i = 0; i < 4; ++i) { mv[i] = mp[2048 + 3072 * i]; mv[4 + i] = mp[1536 + 3072 * i]; tv[i] = mod[k + 3072 * i]; }
        }
        {
#pragma unroll
            for (int i = 0; i < 2; ++i)
                glds16((const char*)x + (size_t)(64 * tp + 16 * h + 8 * i) * 1024 + 128 * np, (lane >> 3) * 1024 + ((lane ^ (lane >> 3)) & 7) * 16,
                       l0 + M_R + (2 * h + i) * 1024);
            if (h < 3) {
                const float v = rbf((tv[0] + tv[1]) + (tv[2] + tv[3]));
                *(LAS float*)((LAS char*)lds + M_TAB + h * 256 + lane * 4) = h == 1 ? rbf(1.f + v) : v;
            }
            LAS char* sc = (LAS char*)lds + (h < 2 ? M_T : M_X1) + (h & 1) * 4096;
            const LAS char* tr = sc + (4 * hh + ((lane & 15) >> 2)) * 64 + (lane & 16) * 2 + (lane & 3) * 8;
            u32x4 f0[4];
#pragma unroll
            for (int i = 0; i < 4; ++i) *(LAS u32x4*)(sc + i * 1024 + l16) = rw[i];
            asm volatile("" ::: "memory");
#pragma unroll
            for (int i = 0; i < 4; ++i) f0[i] = tr_frag(tr + i * 1024);
            if (b < 112) {
                asm volatile("" ::: "memory");
#pragma unroll
                for (int i = 0; i < 4; ++i) *(LAS u32x4*)(sc + i * 1024 + l16) = rw[4 + i];
                LAS u16* bt_ = (LAS u16*)((LAS char*)lds + M_C + h * 256) + (lane >> 4) * 32 + (lane & 15);
                bt_[0] = (u16)(F2U(rbf(1.f + rbf((mv[0] + mv[1]) + (mv[2] + mv[3])))) >> 16); bt_[16] = (u16)(F2U(rbf((mv[4] + mv[5]) + (mv[6] + mv[7]))) >> 16);
                asm volatile("" ::: "memory");
                const unsigned off = WS_WGU + (b & ~1) * GUT + (16 * (b & 1) + h) * 1024 + ((lane >> 4) & 1) * GUT + (lane & 47) * 16;
                f32x16 cgt = {}, cup = {};
#pragma unroll
                for (int i = 0; i < 4; ++i) {
                    const u32x4 f1 = tr_frag(tr + i * 1024);
                    const LAS char* bp = (const LAS char*)lds + M_C + h * 256 + i * 64 + (lane & 1) * 32 + hh * 8;
                    const u32x2 lo = *(const LAS u32x2*)bp, hi = *(const LAS u32x2*)(bp + 16);
                    const u32x4 bc = (lane & 30) == 0 ? u32x4{lo[0], lo[1], hi[0], hi[1]} : u32x4{0u, 0u, 0u, 0u};
                    st16(rs, off + i * 4096, f0[i]); st16(rs, off + i * 4096 + 256, f1);
                    cgt = mfma32(bc, f0[i], cgt); cup = mfma32(bc, f1, cup);
                }
                LAS float* cx = (LAS float*)((LAS char*)lds + M_S);
                if (lane < 32) *(LAS f32x4*)(cx + h * 128 + lane * 4) = f32x4{cgt[0], cgt[1], cup[0], cup[1]};
                int cnt = 0;
                asm volatile("s_waitcnt lgkmcnt(0)" ::: "memory");
                if (lane == 0) cnt = __hip_atomic_fetch_add((LAS int*)(go + 2), 1, __ATOMIC_RELAXED, __HIP_MEMORY_SCOPE_WORKGROUP);
                if (__builtin_amdgcn_readfirstlane(cnt) == 3) {
                    asm volatile("" ::: "memory");
                    f32x2 sm = {0.f, 0.f};
#pragma unroll
                    for (int i = 0; i < 4; ++i) sm += *(const LAS f32x2*)(cx + i * 128 + (lane & 31) * 4 + hh * 2);
                    const float h0 = rbf(sm[0]), h1 = rbf(sm[1]);
                    const unsigned o2 = WS_WGU + ((b & ~1) + ((lane >> 4) & 1)) * GUT + 32768 + ((lane & 15) + 16 * hh) * 16 + 8 * (b & 1);
                    st8(rs, o2, u32x2{pk2(h0, h0), pk2(sm[0] - h0, 0.f)});
                    st8(rs, o2 + 512, u32x2{pk2(h1, h1), pk2(sm[1] - h1, 0.f)});
                }
            } else {
                const unsigned off = (b < 144 ? WS_WO + (4 * h * 32 + b - 112) * 1024 : WS_WD + (4 * h * 112 + b - 144) * 1024) + l16, str = b < 144 ? 32768 : 114688;
#pragma unroll
                for (int i = 0; i < 4; ++i) st16(rs, off + i * str, f0[i]);
            }
            int cnt = 0;
            asm volatile("s_waitcnt vmcnt(0) lgkmcnt(0)" ::: "memory");
            if (lane == 0) cnt = __hip_atomic_fetch_add((LAS int*)(go + 3), 1, __ATOMIC_RELAXED, __HIP_MEMORY_SCOPE_WORKGROUP);
            if (__builtin_amdgcn_readfirstlane(cnt) == 3) {
                arrive(ws, CN_T, 256, FL_T, 8, lane);
                fl_wait<8>(ws, FL_T + g);
                go[1] = 1;
            }
        }
        while (__builtin_amdgcn_readfirstlane(go[1]) == 0) __builtin_amdgcn_s_sleep(8);
        asm volatile("" ::: "memory");
        u32x4 hd[20];
#pragma unroll
        for (int i = 0; i < 20; ++i) hd[i] = ld16d(rs, l16, WS_WD + ((2 * np + (h >> 1)) * 112 + (h & 1) + 2 * i + (i < 8 ? 0 : 72)) * 1024);
        {
            while (__builtin_amdgcn_readfirstlane(go[5]) < 8) __builtin_amdgcn_s_sleep(8);
            asm volatile("" ::: "memory");
            const char* src = ws + WS_WO + (size_t)((2 * np + (h & 1)) * 32 + (h >> 1)) * 1024;
#pragma unroll 4
            for (int i = 0; i < 16; ++i) glds16_dev(src + i * 2048, l16, l0 + i * 4096 + h * 1024);
        }
        WAITV_BAR(0);
        handoff<8>(ws, CN_O + 8 * qb, CN_O + 8 * g + 4 * (i5 & 1), 32, h, lane);
        fetch64<4>(ws + WS_O + (size_t)((2 * tp + (h & 1)) * 32 + (h >> 1)) * 1024, l0, h, l16);
        BARE(); BARE();
        handoff(ws, CN_Y + 4 * (tp >> 1), CN_Y + 4 * rp, 16, h, lane);
        {
            const char* src = ws + WS_Y + (size_t)(rp * 4 + h) * 32768;
#pragma unroll
            for (int u = 0; u < 32; ++u) {
                glds16_dev(src + u * 1024, l16, l0 + u * 4096 + h * 1024);
                if (u == 7) WAITV_BAR(6);
                if (u == 11) WAITV_BAR(8);
                if (u == 15) WAITV_BAR(8);
                if (u == 25) WAITV_BAR(8);
            }
            WAITV_BAR(0);
#pragma unroll
            for (int i = 0; i < 20; ++i) {
                *(LAS u32x4*)((LAS char*)lds + M_WD(i >> 1) + (i & 1) * 4096 + h * 1024 + l16) = hd[i];
                asm volatile("" ::: "memory");
                __builtin_amdgcn_s_sleep(3);
            }
            WAITV_BAR(0);
        }
        handoff(ws, CN_C + 4 * rp, CN_C + 4 * (tp >> 1), 16, h, lane);
        fetch64<14>(ws + WS_ACT + (size_t)((2 * tp + (h & 1)) * 112 + (h >> 1)) * 1024, l0, h, l16);
        return;
    }
    const int kh = w >> 2, ca = (w >> 1) & 1, bt = w & 1;
    const unsigned lx = l16 ^ (kh << 8);
    u32x4 qf;
    const unsigned l2 = l16;
    const bool dl = (l2 & 256) == 0;
    const int cst = (l2 & 176) == 0 ? 2048 : 2064;
    const LAS char* kp = (const LAS char*)lds + l2;
    const LAS char* vp0 = (const LAS char*)lds + (dl ? 1024 + ((l2 >> 1) & 256) + (l2 & 240) : cst);
    const LAS char* vp1 = (const LAS char*)lds + (dl ? 1536 + ((l2 >> 1) & 256) + (l2 & 240) : cst);
    f32x16 o = {};
    {
        constexpr int XW = 16, X0 = 6, X1 = 4;
        u32x4 xf[XW];
        const rsrc_t xs = mk_rsrc(ws + WS_XH + (size_t)(8 * qb + w) * 32768, 32768);
        float sa[4], cv[4];
#pragma unroll
        for (int i = 0; i < 4; ++i) { sa[i] = mod[3072 * i + tid]; cv[i] = mod[3072 * i + 512 + tid]; }
        const u32x4 wr = ld16(rs, l16, WS_WQKV + head * 49152 + w * 1024);
        BARE();
        xf[0] = ld16(xs, l16, 0);
        SB();
        LAS float* tab = (LAS float*)((LAS char*)lds + P1_T) + ((tid >> 4) * 2 + ((tid >> 2) & 1)) * 16 + ((tid >> 3) & 1) * 4 + (tid & 3);
        tab[0] = rbf(1.f + rbf((cv[0] + cv[1]) + (cv[2] + cv[3]))); tab[8] = rbf((sa[0] + sa[1]) + (sa[2] + sa[3]));
        unsigned to = P1_T + w3 * 256 + gs_off(w - 3 * w3, l16); PIN(to);
        BAR();
#pragma unroll
        for (int u = 1; u < X1; ++u) xf[u] = ld16(xs, l16, u * 1024);
        scale_first(lds, wr, w, l16, to);
        SB();
#pragma unroll
        for (int u = X1; u < X0; ++u) xf[u] = ld16(xs, l16, u * 1024);
        BAR();
        f32x16 aq = {}; f32x4 av[2] = {};
        const LAS char* wb = (const LAS char*)lds + P1_W + l16;
        const LAS char* wc = (const LAS char*)lds + P1_W + 1024 + ((l16 & 256) ? 1536 : 0) + ((l16 >> 1) & 256) + (l16 & 240);
        u32x4 wq[2], wv;
        wq[0] = LDS16(wb); wq[1] = LDS16(wb + 1536); wv = LDS16(wc);
        f32x4 q0, q1, k0, k1; float cvv;
#pragma unroll
        for (int u = 0; u < 32; u += 2) {
            if (u == 20) { PIN(o); PIN(kp); PIN(vp0); PIN(vp1); }
            if (u == 28) {
                wait_go(go + 6, 3);
                const LAS char* cb_ = (const LAS char*)lds + M_C + ((l16 >> 5) & 16);
                q0 = *(const LAS f32x4*)cb_; q1 = *(const LAS f32x4*)(cb_ + 32); k0 = *(const LAS f32x4*)(cb_ + 64); k1 = *(const LAS f32x4*)(cb_ + 96);
                cvv = *(const LAS float*)((const LAS char*)lds + M_C + 128 + ((l16 & 240) >> 2));
            }
            if (u == 6 || u == 14) BARE();
            aq = mfma32(wq[0], xf[u % XW], aq); if (u < 30) wq[0] = LDS16(wb + (u + 2) * 1536); SB();
            aq = mfma32(wq[1], xf[(u + 1) % XW], aq); if (u < 30) wq[1] = LDS16(wb + (u + 3) * 1536); SB();
            u32x4 ta, tb_;
#pragma unroll
            for (int j = 0; j < 4; ++j) { auto r = __builtin_amdgcn_permlane16_swap(xf[u % XW][j], xf[(u + 1) % XW][j], false, false); ta[j] = r[0]; tb_[j] = r[1]; }
            av[0] = mfma16(ta, wv, av[0]); av[1] = mfma16(tb_, wv, av[1]); if (u < 30) wv = LDS16(wc + (u + 2) * 1536);
            SB();
#pragma unroll
            for (int e = u; e < u + 2; ++e) {
                if (e < XW - X0) { xf[(X0 + 2 * e) % XW] = ld16(xs, l16, (X0 + 2 * e) * 1024); xf[(X0 + 1 + 2 * e) % XW] = ld16(xs, l16, (X0 + 1 + 2 * e) * 1024); }
                else if (e + XW < 32) xf[e % XW] = ld16(xs, l16, (e + XW) * 1024);
            }
            SB();
        }
        const u32x4 kf = {pk2(aq[8] + k0[0], aq[9] + k0[1]), pk2(aq[10] + k0[2], aq[11] + k0[3]), pk2(aq[12] + k1[0], aq[13] + k1[1]), pk2(aq[14] + k1[2], aq[15] + k1[3])};
        const unsigned kvo = WS_KV + (head * 64 + 8 * qb + w) * 2048;
        st16(rs, kvo + l16, kf);
        *(LAS u32x4*)((LAS char*)lds + w * KVT + l16) = kf;
        {
            auto r0 = __builtin_amdgcn_permlane32_swap(pk2(av[0][0] + cvv, av[0][1] + cvv), pk2(av[1][0] + cvv, av[1][1] + cvv), false, false);
            auto r1 = __builtin_amdgcn_permlane32_swap(pk2(av[0][2] + cvv, av[0][3] + cvv), pk2(av[1][2] + cvv, av[1][3] + cvv), false, false);
            const u32x4 vf = {r0[0], r1[0], r0[1], r1[1]};
            st16(rs, kvo + 1024 + l16, vf);
            *(LAS u32x4*)((LAS char*)lds + w * KVT + 1024 + l16) = vf;
        }
        const float qs = 0.25f * 1.4426950408889634f;
        qf = u32x4{pk2((aq[0] + q0[0]) * qs, (aq[1] + q0[1]) * qs), pk2((aq[2] + q0[2]) * qs, (aq[3] + q0[3]) * qs),
                   pk2((aq[4] + q1[0]) * qs, (aq[5] + q1[1]) * qs), pk2((aq[6] + q1[2]) * qs, (aq[7] + q1[3]) * qs)};
    }
    const int nt4 = cg * 7 + w;
    const unsigned wo5 = WS_WD + ((2 * np + ca) * 112 + 2 * kh + bt) * 1024;
    const unsigned wo4 = w < 7 ? WS_WGU + nt4 * GUT : wo5 + 4 * 4096, ws4 = w < 7 ? 1024 : 4096;
    const unsigned lw = w < 7 ? l16 : lx;
    constexpr int WW = 14;
    u32x4 wf[WW];
    {
        BAR();
        f32x16 nm, sA, sB;
        {
            sA = mfma32(LDS16(kp), qf, o);
            float m = __builtin_fmaxf(__builtin_fmaxf(sA[0], sA[1]), sA[2]), m2 = __builtin_fmaxf(__builtin_fmaxf(sA[3], sA[4]), sA[5]);
#pragma unroll
            for (int r = 6; r < 14; r += 4) { m = __builtin_fmaxf(__builtin_fmaxf(m, sA[r]), sA[r + 1]); m2 = __builtin_fmaxf(__builtin_fmaxf(m2, sA[r + 2]), sA[r + 3]); }
            m = __builtin_fmaxf(__builtin_fmaxf(m, sA[14]), __builtin_fmaxf(m2, sA[15]));
            m = -hmax(m);
#pragma unroll
            for (int r = 0; r < 16; ++r) { nm[r] = m; sA[r] += m; }
        }
        u32x4 kA = LDS16(kp + KVT), vA0 = LDS16(vp0), vA1 = {0u, 0u, 0u, 0u}, kB, vB0, vB1, p0, p1 = {0u, 0u, 0u, 0u};
        SB();
#define EX2(C, i) C[i] = EX(C[i]); C[i + 1] = EX(C[i + 1])
#define PK4(P, C, i) P[0] = pk2(C[i], C[i + 1]); P[1] = pk2(C[i + 2], C[i + 3]); P[2] = pk2(C[i + 4], C[i + 5]); P[3] = pk2(C[i + 6], C[i + 7])
#define TILE(C, N, u, K, V0, V1, KN, V0N, V1N) do { \
        N = mfma32(K, qf, nm); SB(); \
        V0N = LDS16(vp0 + ((u) + 1) * KVT); V1N = LDS16(vp1 + (u) * KVT); KN = LDS16(kp + ((u) + 2) * KVT); SB(); \
        EX2(C, 0); EX2(C, 2); EX2(C, 4); SB(); \
        o = mfma32(V1, p1, o); SB(); \
        EX2(C, 6); SB(); PK4(p0, C, 0); SB(); EX2(C, 8); EX2(C, 10); SB(); \
        o = mfma32(V0, p0, o); SB(); \
        EX2(C, 12); EX2(C, 14); SB(); PK4(p1, C, 8); SB(); } while (0)
#define TILE2(u) do { TILE(sA, sB, u, kA, vA0, vA1, kB, vB0, vB1); TILE(sB, sA, (u) + 1, kB, vB0, vB1, kA, vA0, vA1); } while (0)
#define ADV(n) do { kp += (n) * KVT; vp0 += (n) * KVT; vp1 += (n) * KVT; } while (0)
        PIN(sA); PIN(nm); PIN(p1); PIN(vA1);
        asm volatile("s_waitcnt vmcnt(0)" ::: "memory");
        if (l2 == 0) __hip_atomic_fetch_add((LAS int*)(go + 4), 1, __ATOMIC_RELAXED, __HIP_MEMORY_SCOPE_WORKGROUP);
#pragma unroll 1
        for (int it = 0; it < 7; ++it) {
            if (it == 1) {
                BARE();
                sA = mfma32(LDS16(kp), qf, nm); kA = LDS16(kp + KVT); vA0 = LDS16(vp0);
            }
            if (it == 2) BARE();
            if (it == 5 && l2 == 0) __hip_atomic_fetch_add((LAS int*)(go + 5), 1, __ATOMIC_RELAXED, __HIP_MEMORY_SCOPE_WORKGROUP);
            if (it < 6) __builtin_amdgcn_s_setprio(2); else __builtin_amdgcn_s_setprio(1);
            TILE2(0); TILE2(2);
            asm volatile("s_cmp_gt_u32 %0, 1\n\ts_cbranch_scc1 .Lp1_%=\n\ts_setprio 1\n.Lp1_%=:" :: "s"(it) : "scc");
            TILE2(4);
            asm volatile("s_cmp_gt_u32 %0, 1\n\ts_cbranch_scc1 .Lp0_%=\n\ts_setprio 0\n.Lp0_%=:" :: "s"(it) : "scc");
            TILE2(6); PIN(p1); SB(); ADV(8);
        }
        wait_go(go + 1, 1);
#pragma unroll
        for (int u = 0; u < WW; ++u) wf[u] = ld16d(rs, lw, wo4 + u * ws4);
        SB();
        TILE2(0); TILE2(2);
        __builtin_amdgcn_s_setprio(0);
        TILE2(4); TILE2(6);
        o = mfma32(vA1, p1, o);
        bool bad = !(o[8] <= 1.0e12f);
#pragma unroll
        for (int r = 0; r < 8; ++r) bad |= !(__builtin_fabsf(o[r]) <= 3.0e38f);
        if (__builtin_amdgcn_ballot_w64(bad) != 0) {
            const unsigned kv0 = WS_KV + head * 131072, vo = 1024 + ((l2 >> 1) & 256) + (l2 & 240), cw = (l2 & 176) == 0 ? 0x3F803F80u : 0u;
            const u32x4 cf = {cw, cw, cw, cw};
            const f32x16 z = {};
            float mx = -__builtin_inff();
#pragma unroll 1
            for (int t = 0; t < 64; ++t) {
                const f32x16 s0 = mfma32(ld16d(rs, l2, kv0 + t * 2048), qf, z);
#pragma unroll
                for (int r = 0; r < 16; ++r) mx = __builtin_fmaxf(mx, s0[r]);
            }
            mx = hmax(mx);
            o = z;
#pragma unroll 1
            for (int t = 0; t < 64; ++t) {
                const u32x4 e0 = ld16d(rs, vo, kv0 + t * 2048), e1 = ld16d(rs, vo + 512, kv0 + t * 2048);
                const f32x16 s0 = mfma32(ld16d(rs, l2, kv0 + t * 2048), qf, z);
#pragma unroll
                for (int j = 0; j < 4; ++j) {
                    p0[j] = pk2(EX(s0[2 * j] - mx), EX(s0[2 * j + 1] - mx));
                    p1[j] = pk2(EX(s0[8 + 2 * j] - mx), EX(s0[8 + 2 * j + 1] - mx));
                }
                o = mfma32(dl ? e0 : cf, p0, o);
                o = mfma32(dl ? e1 : cf, p1, o);
            }
        }
        const float rl = __builtin_amdgcn_rcpf(o[8]);
        st16s(rs, l2, WS_O + ((qb * 8 + w) * 32 + head) * 1024,
              u32x4{pk2(o[0] * rl, o[1] * rl), pk2(o[2] * rl, o[3] * rl), pk2(o[4] * rl, o[5] * rl), pk2(o[6] * rl, o[7] * rl)});
        WAITV_BAR(0);
    }
    {
        unsigned l3 = l16; PIN(l3);
        u32x4 wa[16];
#pragma unroll
        for (int i = 0; i < 16; ++i) wa[i] = LDS16((const LAS char*)lds + kh * 2048 + ca * 1024 + (l3 ^ (kh << 8)) + i * 4096);
        const LAS char* bp = (const LAS char*)lds + 65536 + kh * 2048 + bt * 1024 + l3;
        f32x16 acc = {};
#pragma unroll
        for (int st = 0; st < 4; ++st) {
            BAR();
            u32x4 bb[4];
#pragma unroll
            for (int i = 0; i < 4; ++i) bb[i] = LDS16(bp + (4 * st + i) * 4096);
            SB();
#pragma unroll
            for (int i = 0; i < 4; ++i) acc = mfma32(wa[4 * st + i], bb[i], acc);
            SB();
        }
        const LAS char* tb = (const LAS char*)lds + M_TAB + (32 * ca + 16 * kh) * 4 + ((l3 >> 5) & 16);
        const f32x4 ga0 = *(const LAS f32x4*)tb, ga1 = *(const LAS f32x4*)(tb + 32), g20 = *(const LAS f32x4*)(tb + 256), g21 = *(const LAS f32x4*)(tb + 288);
        const int c0 = 16 * (4 * ca + 2 * kh);
        const LAS char* xp = (const LAS char*)lds + M_R + 4096 * bt + ((l3 & 496) << 3) + ((l3 >> 6) & 8);
        const u32x2 xr0 = *(const LAS u32x2*)(xp + ((c0 ^ l3) & 112)), xr1 = *(const LAS u32x2*)(xp + (((c0 + 16) ^ l3) & 112));
        SB();
        *(LAS f32x4*)((LAS char*)lds + (w ^ 4) * 2048 + l3) = F4(acc, 8); *(LAS f32x4*)((LAS char*)lds + (w ^ 4) * 2048 + 1024 + l3) = F4(acc, 12);
        BAR();
        const f32x4 u0 = *(const LAS f32x4*)((const LAS char*)lds + w * 2048 + l3), u1 = *(const LAS f32x4*)((const LAS char*)lds + w * 2048 + 1024 + l3);
        float a[8];
#pragma unroll
        for (int r = 0; r < 4; ++r) { a[r] = acc[r] + u0[r]; a[4 + r] = acc[4 + r] + u1[r]; }
        const u32x4 x1p = {pk2(__builtin_fmaf(ga0[0], a[0], bflo(xr0[0])), __builtin_fmaf(ga0[1], a[1], bfhi(xr0[0]))), pk2(__builtin_fmaf(ga0[2], a[2], bflo(xr0[1])), __builtin_fmaf(ga0[3], a[3], bfhi(xr0[1]))),
                           pk2(__builtin_fmaf(ga1[0], a[4], bflo(xr1[0])), __builtin_fmaf(ga1[1], a[5], bfhi(xr1[0]))), pk2(__builtin_fmaf(ga1[2], a[6], bflo(xr1[1])), __builtin_fmaf(ga1[3], a[7], bfhi(xr1[1])))};
#pragma unroll
        for (int r = 0; r < 4; ++r) { a[2 * r] = bflo(x1p[r]); a[2 * r + 1] = bfhi(x1p[r]); }
        st16s(rs, l3, WS_Y + ((2 * tp + bt) * 32 + 4 * np + 2 * ca + kh) * 1024,
              u32x4{pk2(a[0] * g20[0], a[1] * g20[1]), pk2(a[2] * g20[2], a[3] * g20[3]), pk2(a[4] * g21[0], a[5] * g21[1]), pk2(a[6] * g21[2], a[7] * g21[3])});
        *(LAS u32x4*)((LAS char*)lds + M_X1 + w * 1024 + l3) = x1p;
        const float mean = (((a[0] + a[1]) + (a[2] + a[3])) + ((a[4] + a[5]) + (a[6] + a[7]))) * 0.125f;
        float q2 = 0.f;
#pragma unroll
        for (int r = 0; r < 8; ++r) { const float d = a[r] - mean; q2 = __builtin_fmaf(d, d, q2); }
        auto m2 = __builtin_amdgcn_permlane32_swap(F2U(mean), F2U(mean), false, false);
        const float ml = U2F(m2[0]), mh = U2F(m2[1]), d = ml - mh;
        q2 = __builtin_fmaf(4.f * d, d, hsum(q2));
        if (l3 < 512) *(LAS f32x2*)((LAS char*)lds + M_S + (bt * 4 + (w >> 1)) * 256 + (l3 >> 1)) = f32x2{0.5f * (ml + mh), q2};
        WAITV_BAR(0);
    }
    u32x4 wd[22];
    if (w < 7) {
        f32x16 acc[4] = {};
        const LAS char* yb = (const LAS char*)lds + l16;
        __builtin_amdgcn_s_setprio(1);
        BARE();
        u32x4 yf[4];
#pragma unroll
        for (int t = 0; t < 4; ++t) yf[t] = LDS16(yb + t * 1024);
#pragma unroll
        for (int u = 0; u < 33; ++u) {
            if (u == 1 || u == 3 || u == 7 || u == 17) BARE();
            if (u == 31) wait_go(go, 2);
            if (u == 15 || u == 31) { yb += 65536; PIN(yb); }
#pragma unroll
            for (int t = 0; t < 4; ++t) { acc[t] = mfma32(wf[u % WW], yf[t], acc[t]); if (u < 32) yf[t] = LDS16(yb + (((u + 1) & 15) * 4 + t) * 1024); SB(); }
#pragma unroll
            for (int i = 0; i < 33 - WW; ++i)
                if (u == (i < 10 ? (8 * i + 9) / 5 : i + 7)) wf[i % WW] = ld16d(rs, l16, wo4 + (i + WW) * 1024);
            if (u + WW >= 33) wd[u + WW - 29] = ld16d(rs, lx, wo5 + (u + WW - 29) * 4096);
            SB();
        }
#pragma unroll
        for (int j = WW + 4; j < 22; ++j) wd[j] = ld16d(rs, lx, wo5 + j * 4096);
        SB();
        __builtin_amdgcn_s_setprio(0);
        unsigned lc = l16; PIN(lc);
#pragma unroll
        for (int t = 0; t < 4; ++t) {
            const f32x2 rr = *(const LAS f32x2*)((const LAS char*)lds + M_C + 256 * t + ((lc >> 1) & 248));
            float a[8];
#pragma unroll
            for (int r = 0; r < 8; ++r) a[r] = acc[t][r] * acc[t][8 + r] * __builtin_amdgcn_rcpf(__builtin_fmaf(EX(acc[t][r] * rr[0]), rr[1], rr[1]));
            st16s(rs, lc, WS_ACT + ((4 * rp + t) * 112 + nt4) * 1024, u32x4{pk2(a[0], a[1]), pk2(a[2], a[3]), pk2(a[4], a[5]), pk2(a[6], a[7])});
            SB();
        }
        m_p5(lds, ws, out, wd, w, tp, np, l16);
    } else {
        {
            const LAS f32x2* sp = (const LAS f32x2*)((const LAS char*)lds + M_S + ((l16 & 512) << 1) + ((l16 & 496) >> 1));
            const f32x2 p0 = sp[0], p1 = sp[32], p2 = sp[64], p3 = sp[96];
            const float m = 0.25f * ((p0[0] + p1[0]) + (p2[0] + p3[0]));
            const float d0 = p0[0] - m, d1 = p1[0] - m, d2 = p2[0] - m, d3 = p3[0] - m;
            const float q = ((p0[1] + p1[1]) + (p2[1] + p3[1])) + 16.f * ((d0 * d0 + d1 * d1) + (d2 * d2 + d3 * d3));
            st8(rs, WS_STAT + (2 * tp * 8 + np) * 256 + ((l16 & 512) << 2) + ((l16 & 496) >> 1), u32x2{F2U(m), F2U(q)});
            asm volatile("s_waitcnt vmcnt(0)" ::: "memory");
            arrive(ws, CN_S + (tp >> 1), 16, FL_S + (tp >> 1) * 4, 2, lane);
        }
        BARE(); BARE(); BARE(); BARE();
        fl_wait<2>(ws, FL_S + rp * 4 + (g & 1));
        u32x2 sv[16];
#pragma unroll
        for (int i = 0; i < 16; ++i)
            sv[i] = __builtin_amdgcn_raw_buffer_load_b64(rs, ((l16 & 512) << 2) + ((l16 & 496) >> 1), WS_STAT + rp * 8192 + (i >> 3) * 4096 + (i & 7) * 256, 16);
        BARE();
#pragma unroll
        for (int p = 0; p < 2; ++p) {
            float s = 0.f;
#pragma unroll
            for (int i = 0; i < 8; ++i) s += U2F(sv[8 * p + i][0]);
            const float mean = s * 0.125f;
            float q = 0.f;
#pragma unroll
            for (int i = 0; i < 8; ++i) { const float d = U2F(sv[8 * p + i][0]) - mean; q += __builtin_fmaf(64.f * d, d, U2F(sv[8 * p + i][1])); }
            const float var = q * (1.f / 512.f) + 1e-5f, rq = __builtin_amdgcn_rsqf(var), sg = var * rq;
            const float mh = rbf(-mean), sh = rbf(sg);
            const unsigned m0 = pk2(mh, -mean - mh), m1 = pk2(mh, 0.f), s0 = pk2(sh, sg - sh), s1 = pk2(sh, 0.f);
            LAS char* d = (LAS char*)lds + (128 + 2 * p) * 1024 + ((l16 & 512) << 1) + (l16 & 496);
            *(LAS u32x4*)d = u32x4{m0, m1, m0, m1}; *(LAS u32x4*)(d + 512) = u32x4{s0, s1, s0, s1};
            *(LAS f32x2*)((LAS char*)lds + M_C + 512 * p + (l16 >> 1)) = f32x2{-1.4426950408889634f * rq, var};
        }
        asm volatile("s_waitcnt lgkmcnt(0)" ::: "memory");
        go[0] = 2;
#pragma unroll
        for (int j = 4; j < 22; ++j) wd[j] = j < 4 + WW ? wf[j - 4] : ld16d(rs, lx, wo5 + j * 4096);
        m_p5(lds, ws, out, wd, 7, tp, np, l16);
    }
}

extern "C" void kernel_launch(void* const* d_in, const int* in_sizes, int n_in, void* d_out, int out_size, void* d_ws, size_t ws_size,
                              hipStream_t stream) {
    const u16* x = (const u16*)d_in[0];
    const u16* wqkv = (const u16*)d_in[1];
    const u16* wo = (const u16*)d_in[2];
    const u16* wg = (const u16*)d_in[4];
    const u16* wu = (const u16*)d_in[5];
    const u16* wd = (const u16*)d_in[6];
    const u16* c = (const u16*)d_in[7];
    const u16* wmod = (const u16*)d_in[8];
    char* ws = (char*)d_ws;
    (void)hipFuncSetAttribute((const void*)m_main, hipFuncAttributeMaxDynamicSharedMemorySize, M_LDS);
    k0_prep<<<256, 1024, 0, stream>>>(x, wqkv, c, wmod, ws);
    m_main<<<256, 768, M_LDS, stream>>>(ws, x, wo, wg, wu, wd, (u16*)d_out);
}
```
